```python
import torch

def _softmax(x, axis=-1):
    return torch.softmax(x, dim=axis)
def _gelu(x):
    return torch.nn.functional.gelu(x, approximate="tanh")
def _silu(x):
    return torch.nn.functional.silu(x)
def _sigmoid(x):
    return torch.sigmoid(x)
def _layernorm(x, axis=-1, eps=1e-5):
    m = x.mean(dim=axis, keepdim=True)
    v = x.var(dim=axis, keepdim=True, unbiased=False)
    return (x - m) / torch.sqrt(v + eps)
def _scatter_add(tgt, idx, src, axis=0):
    return tgt.clone().index_add_(axis, idx, src)
def _segment_sum(src, seg, n):
    out = torch.zeros((n,) + src.shape[1:], dtype=src.dtype)
    return out.index_add_(0, seg, src)
def _pad(x, pw):
    flat = tuple(v for pair in reversed(pw) for v in pair)
    return torch.nn.functional.pad(x, flat)

kernel_name = 'recurrent_scan__sliding_local_dilated__glu_gated_reglu__prompt'

def setup_inputs():

    _w = (lambda a: (lambda t: t.to(torch.bfloat16) if t.is_floating_point() else t)(torch.as_tensor(a)))
    def _A_setup():
        B, S, D = 1, 2048, 512
        return {
            "x": _w((lambda *s: torch.randn(*s, dtype=torch.bfloat16) * 0.05)(B, S, D)),
            "wqkv": _w((lambda *s: torch.randn(*s, dtype=torch.bfloat16) * 0.2)(D, 3 * D)),
            "wo": _w((lambda *s: torch.randn(*s, dtype=torch.bfloat16) * 0.2)(D, D)),
        }
    _A_in = _A_setup()

    def _B_setup():
        B, S, D, F = 1, 2048, 512, 1792
        return {
            "x": _w((lambda *s: torch.randn(*s, dtype=torch.bfloat16) * 0.05)(B, S, D)),
            "W_gate": _w((lambda *s: torch.randn(*s, dtype=torch.bfloat16) * 0.1)(D, F) * (2.0 / D) ** 0.5),
            "W_up": _w((lambda *s: torch.randn(*s, dtype=torch.bfloat16) * 0.1)(D, F) * (2.0 / D) ** 0.5),
            "W_down": _w((lambda *s: torch.randn(*s, dtype=torch.bfloat16) * 0.1)(F, D) * (1.0 / F) ** 0.5),
        }
    _B_in = _B_setup()

    return {
        "A_x": _A_in["x"],
        "A_wqkv": _A_in["wqkv"],
        "A_wo": _A_in["wo"],
        "B_x": _B_in["x"],
        "B_W_gate": _B_in["W_gate"],
        "B_W_up": _B_in["W_up"],
        "B_W_down": _B_in["W_down"],
    }

def reference(A_x, A_wqkv, A_wo, B_x, B_W_gate, B_W_up, B_W_down):
    def _A_ref(x, wqkv, wo):
        B, S, D = x.shape
        H = 32; Dh = D // H
        W, dil = 8, 2
        qkv = (lambda x, *d: x.permute(*d))((x @ wqkv).reshape(B, S, 3, H, Dh), 2, 0, 3, 1, 4)
        q, k, v = qkv[0], qkv[1], qkv[2]
        scale = Dh ** -0.5
        pos = torch.arange(S)
        off = torch.arange(W) * dil
        idx = pos[:, None] - off[None, :]
        mask = idx >= 0
        idx_c = torch.where(mask, idx, 0).reshape(-1)
        kg = k[:, :, idx_c, :].reshape(B, H, S, W, Dh)
        vg = v[:, :, idx_c, :].reshape(B, H, S, W, Dh)
        logits = torch.einsum("bhsd,bhswd->bhsw", q, kg) * scale
        logits = torch.where(mask[None, None, :, :], logits, -1e9)
        attn = _softmax(logits, axis=-1)
        o = torch.einsum("bhsw,bhswd->bhsd", attn, vg)
        return (lambda x, *d: x.permute(*d))(o, 0, 2, 1, 3).reshape(B, S, D) @ wo

    def _B_ref(x, W_gate, W_up, W_down):
        gate = torch.einsum("bsd,df->bsf", x, W_gate)
        up = torch.einsum("bsd,df->bsf", x, W_up)
        gate = gate * (lambda x, dt: x.to(dt))(gate > 0, gate.dtype)
        h = gate * up
        return torch.einsum("bsf,fd->bsd", h, W_down)

    x = A_x
    xn = _layernorm(x, axis=-1)
    s = _A_ref(xn, A_wqkv, A_wo)
    g = _silu(_B_ref(xn, B_W_gate, B_W_up, B_W_down))
    return x + s * g
```

```cpp
#include <hip/hip_runtime.h>
#include <hip/hip_bf16.h>
#include <stdint.h>

typedef unsigned short u16;
__device__ __forceinline__ float bf2f(u16 b) { return __uint_as_float(((unsigned)b) << 16); }
__device__ __forceinline__ u16 f2bf(float f) {
    unsigned u = __float_as_uint(f);
    u += 0x7FFF + ((u >> 16) & 1);
    return (u16)(u >> 16);
}
__device__ __forceinline__ float rbf(float f) { return bf2f(f2bf(f)); }

constexpr int S = 2048, D = 512, F = 1792, H = 32, DH = 16;

__global__ void ln_k(const u16* x, u16* xn) {
    int row = blockIdx.x, t = threadIdx.x;
    float v[8]; float s = 0;
    for (int i = 0; i < 8; ++i) { v[i] = bf2f(x[row * D + t * 8 + i]); s += v[i]; }
    for (int o = 32; o; o >>= 1) s += __shfl_xor(s, o);
    float m = s / D;
    float q = 0;
    for (int i = 0; i < 8; ++i) { float d = v[i] - m; q += d * d; }
    for (int o = 32; o; o >>= 1) q += __shfl_xor(q, o);
    float var = rbf(q / D);
    float mb = rbf(m);
    float sd = rbf(sqrtf(rbf(var + 1e-5f)));
    for (int i = 0; i < 8; ++i) xn[row * D + t * 8 + i] = f2bf(rbf(v[i] - mb) / sd);
}

__global__ void gemm_k(const u16* A, int lda, const u16* B, int ldb, u16* C, int ldc, int M, int N, int K) {
    int n = blockIdx.x * 64 + (threadIdx.x & 63);
    int m = blockIdx.y * 4 + (threadIdx.x >> 6);
    float acc = 0;
    for (int k = 0; k < K; ++k) acc += bf2f(A[m * lda + k]) * bf2f(B[k * ldb + n]);
    C[m * ldc + n] = f2bf(acc);
}

__global__ void attn_k(const u16* qkv, u16* o) {
    int idx = blockIdx.x * 256 + threadIdx.x;
    int s = idx / H, h = idx % H;
    float q[DH];
    for (int d = 0; d < DH; ++d) q[d] = bf2f(qkv[s * 1536 + h * DH + d]);
    float lg[8]; float mx = -1e30f;
    for (int w = 0; w < 8; ++w) {
        int p = s - 2 * w;
        float a;
        if (p >= 0) {
            a = 0;
            for (int d = 0; d < DH; ++d) a += q[d] * bf2f(qkv[p * 1536 + 512 + h * DH + d]);
            a = rbf(rbf(a) * 0.25f);
        } else a = rbf(-1e9f);
        lg[w] = a; mx = fmaxf(mx, a);
    }
    float sum = 0;
    for (int w = 0; w < 8; ++w) { lg[w] = expf(lg[w] - mx); sum += lg[w]; }
    float acc[DH];
    for (int d = 0; d < DH; ++d) acc[d] = 0;
    for (int w = 0; w < 8; ++w) {
        int p = s - 2 * w; if (p < 0) p = 0;
        float a = rbf(lg[w] / sum);
        for (int d = 0; d < DH; ++d) acc[d] += a * bf2f(qkv[p * 1536 + 1024 + h * DH + d]);
    }
    for (int d = 0; d < DH; ++d) o[s * D + h * DH + d] = f2bf(acc[d]);
}

__global__ void h_k(const u16* g, const u16* u, u16* h) {
    int i = blockIdx.x * 256 + threadIdx.x;
    float gv = bf2f(g[i]); gv = gv > 0 ? gv : 0.f;
    h[i] = f2bf(gv * bf2f(u[i]));
}

__global__ void fin_k(const u16* x, const u16* s, const u16* dn, u16* out) {
    int i = blockIdx.x * 256 + threadIdx.x;
    float d = bf2f(dn[i]);
    float g = rbf(d / (1.f + expf(-d)));
    out[i] = f2bf(bf2f(x[i]) + rbf(bf2f(s[i]) * g));
}

extern "C" void kernel_launch(void* const* d_in, const int* in_sizes, int n_in,
                              void* d_out, int out_size, void* d_ws, size_t ws_size,
                              hipStream_t stream) {
    const u16* x = (const u16*)d_in[0];
    const u16* wqkv = (const u16*)d_in[1];
    const u16* wo = (const u16*)d_in[2];
    const u16* wg = (const u16*)d_in[4];
    const u16* wu = (const u16*)d_in[5];
    const u16* wd = (const u16*)d_in[6];
    u16* out = (u16*)d_out;
    u16* ws = (u16*)d_ws;
    u16* xn = ws; ws += S * D;
    u16* qkv = ws; ws += S * 1536;
    u16* o = ws; ws += S * D;
    u16* sb = ws; ws += S * D;
    u16* g = ws; ws += S * F;
    u16* u = ws; ws += S * F;
    u16* h = ws; ws += S * F;
    u16* dn = ws; ws += S * D;
    ln_k<<<S, 64, 0, stream>>>(x, xn);
    gemm_k<<<dim3(1536 / 64, S / 4), 256, 0, stream>>>(xn, D, wqkv, 1536, qkv, 1536, S, 1536, D);
    attn_k<<<S * H / 256, 256, 0, stream>>>(qkv, o);
    gemm_k<<<dim3(D / 64, S / 4), 256, 0, stream>>>(o, D, wo, D, sb, D, S, D, D);
    gemm_k<<<dim3(F / 64, S / 4), 256, 0, stream>>>(xn, D, wg, F, g, F, S, F, D);
    gemm_k<<<dim3(F / 64, S / 4), 256, 0, stream>>>(xn, D, wu, F, u, F, S, F, D);
    h_k<<<S * F / 256, 256, 0, stream>>>(g, u, h);
    gemm_k<<<dim3(D / 64, S / 4), 256, 0, stream>>>(h, F, wd, D, dn, D, S, D, F);
    fin_k<<<S * D / 256, 256, 0, stream>>>(x, sb, dn, out);
}
```

```cpp
#include <hip/hip_runtime.h>
#include <stdint.h>
#include <utility>

typedef unsigned short u16;
typedef __attribute__((ext_vector_type(8))) __bf16 bf16x8;
typedef __attribute__((ext_vector_type(2))) __bf16 bf16x2;
typedef __attribute__((ext_vector_type(4))) short s16x4;
typedef __attribute__((ext_vector_type(8))) short s16x8;
typedef __attribute__((ext_vector_type(4))) float f32x4;
typedef __attribute__((ext_vector_type(2))) float f32x2;
typedef __attribute__((ext_vector_type(4))) unsigned u32x4;
typedef __attribute__((ext_vector_type(2))) unsigned u32x2;
#define LAS __attribute__((address_space(3)))
#define SB() __builtin_amdgcn_sched_barrier(0)

constexpr int S_ = 2048, D_ = 512, F_ = 1792;
constexpr int A2LD = 2304;

template <int... I, class Fn> __device__ __forceinline__ void sfor_impl(std::integer_sequence<int, I...>, Fn&& f) { (f(std::integral_constant<int, I>{}), ...); }
template <int N, class Fn> __device__ __forceinline__ void sfor(Fn&& f) { sfor_impl(std::make_integer_sequence<int, N>{}, f); }

__device__ __forceinline__ float rbf(float f) { return (float)(__bf16)f; }
__device__ __forceinline__ unsigned pk2(float lo, float hi) {
    bf16x2 v; v[0] = (__bf16)lo; v[1] = (__bf16)hi; return __builtin_bit_cast(unsigned, v);
}
__device__ __forceinline__ float bflo(unsigned u) { return __uint_as_float(u << 16); }
__device__ __forceinline__ float bfhi(unsigned u) { return __uint_as_float(u & 0xffff0000u); }

template <int OFF> __device__ __forceinline__ void glds_v(const void* gaddr, unsigned ldsbase) {
    asm volatile("s_add_u32 m0, %1, %2\n\ts_nop 0\n\tglobal_load_lds_dwordx4 %0, off"
                 :: "v"(gaddr), "s"(ldsbase), "n"(OFF) : "memory", "scc");
}
template <int OFF> __device__ __forceinline__ void glds_s(const void* sbase, unsigned voff, unsigned ldsbase) {
    asm volatile("s_add_u32 m0, %2, %3\n\ts_nop 0\n\tglobal_load_lds_dwordx4 %0, %1"
                 :: "v"(voff), "s"(sbase), "s"(ldsbase), "n"(OFF) : "memory", "scc");
}
template <int OFF> __device__ __forceinline__ void glds_vm(const void* gaddr, unsigned ldsbase, unsigned long long mask) {
    asm volatile("s_add_u32 m0, %1, %2\n\ts_mov_b64 exec, %3\n\tglobal_load_lds_dwordx4 %0, off\n\ts_mov_b64 exec, -1"
                 :: "v"(gaddr), "s"(ldsbase), "n"(OFF), "s"(mask) : "memory", "scc");
}
template <int OFF> __device__ __forceinline__ void glds_sm16(const void* sbase, unsigned voff, unsigned ldsbase) {
    asm volatile("s_add_u32 m0, %2, %3\n\ts_mov_b64 exec, 0xffff\n\tglobal_load_lds_dwordx4 %0, %1\n\ts_mov_b64 exec, -1"
                 :: "v"(voff), "s"(sbase), "s"(ldsbase), "n"(OFF) : "memory", "scc");
}
template <int N> __device__ __forceinline__ void wait_bar() {
    __builtin_amdgcn_s_waitcnt(0xC07F);
    asm volatile("s_waitcnt vmcnt(%0)\n\ts_barrier" :: "n"(N) : "memory");
}
__device__ __forceinline__ void bar_lgkm() { __builtin_amdgcn_s_waitcnt(0xC07F); asm volatile("s_barrier" ::: "memory"); }

__device__ __forceinline__ bf16x8 lds_a(unsigned addr) { return *(const LAS bf16x8*)(uintptr_t)addr; }
__device__ __forceinline__ bf16x8 lds_b(unsigned addr) {
    s16x4 lo = __builtin_amdgcn_ds_read_tr16_b64_v4i16((LAS s16x4*)(uintptr_t)addr);
    s16x4 hi = __builtin_amdgcn_ds_read_tr16_b64_v4i16((LAS s16x4*)(uintptr_t)(addr + 512));
    s16x8 r = __builtin_shufflevector(lo, hi, 0, 1, 2, 3, 4, 5, 6, 7);
    return __builtin_bit_cast(bf16x8, r);
}
__device__ __forceinline__ f32x4 mfma32(bf16x8 a, bf16x8 b, f32x4 c) { return __builtin_amdgcn_mfma_f32_16x16x32_bf16(a, b, c, 0, 0, 0); }
__device__ __forceinline__ float dot1(unsigned t, float acc) {
    return __builtin_amdgcn_fdot2_f32_bf16(__builtin_bit_cast(bf16x2, t), __builtin_bit_cast(bf16x2, 0x3f803f80u), acc, false);
}
__device__ __forceinline__ float dots(unsigned t, float acc) {
    return __builtin_amdgcn_fdot2_f32_bf16(__builtin_bit_cast(bf16x2, t), __builtin_bit_cast(bf16x2, t), acc, false);
}
__device__ __forceinline__ unsigned word(bf16x8 v, int i) { u32x4 u = __builtin_bit_cast(u32x4, v); unsigned t = u[i]; return t; }
__device__ __forceinline__ float xmax16(float v) { auto r = __builtin_amdgcn_permlane16_swap(__float_as_uint(v), __float_as_uint(v), false, false); return fmaxf(__uint_as_float(r[0]), __uint_as_float(r[1])); }
__device__ __forceinline__ float xmax32(float v) { auto r = __builtin_amdgcn_permlane32_swap(__float_as_uint(v), __float_as_uint(v), false, false); return fmaxf(__uint_as_float(r[0]), __uint_as_float(r[1])); }
__device__ __forceinline__ float xadd16(float v) { auto r = __builtin_amdgcn_permlane16_swap(__float_as_uint(v), __float_as_uint(v), false, false); return __uint_as_float(r[0]) + __uint_as_float(r[1]); }
__device__ __forceinline__ float xadd32(float v) { auto r = __builtin_amdgcn_permlane32_swap(__float_as_uint(v), __float_as_uint(v), false, false); return __uint_as_float(r[0]) + __uint_as_float(r[1]); }

constexpr int NB = 5, NA = 3;
constexpr int BBLK = 20480, ABLK = 18432;
constexpr int L_B = 0, L_A = NB * BBLK, L_ST = L_A + NA * ABLK;
constexpr int L_ROW = L_ST, L_COL = L_ST + 144 * 8;
constexpr int K1_LDS = L_COL + 8 * 80 * 4;

template <bool TA>
__device__ __forceinline__ void k1_body(const u16* __restrict__ x, const u16* __restrict__ wqkv, const u16* __restrict__ wg,
                                        const u16* __restrict__ wu, u16* __restrict__ a2, int p, int j0, unsigned lbase) {
    const int tid = threadIdx.x, l = tid & 63;
    const int w = __builtin_amdgcn_readfirstlane(tid >> 6);
    const int m = w >> 2, j = w & 3;
    const int r0 = p * 128;
    constexpr int NAI = TA ? 3 : 2;

    const char* xb = (const char*)x + (size_t)r0 * 1024;
    unsigned voffA, voffH = 0;
    {
        int ir = 16 + 8 * w + (l >> 3);
        int ch = (l & 7) ^ ((ir >> 1) & 7);
        voffA = (unsigned)((ir - 16) * 1024 + ch * 16);
        if (TA) {
            int hr = 2 * w + ((l >> 3) & 1);
            int hch = (l & 7) ^ ((hr >> 1) & 7);
            int gr = r0 - 16 + hr; gr = gr < 0 ? 0 : gr;
            voffH = (unsigned)(gr * 1024 + hch * 16);
        }
    }
    const char* gB[3]; unsigned incB[3];
#pragma unroll
    for (int i = 0; i < 3; ++i) {
        const int G = (i == 2) ? 4 : (2 * i + (w >> 2));
        const int at = (i == 2) ? (w >> 1) : (w & 3);
        const int k = 8 * at + (l >> 3);
        const int lch = (l & 7) ^ ((((k >> 1) & 1) | (((k >> 3) & 1) << 1)) << 1);
        const int ct = lch >> 1, half = lch & 1;
        const u16* base; int stride, col;
        if (TA) {
            if (G < 3) { base = wqkv; stride = 1536; col = G * 512 + 64 * j0 + ct * 16; }
            else { base = (G == 3) ? wg : wu; stride = F_; col = 64 * j0 + ct * 16; }
        } else {
            const int jj = j0 - 8, f0 = 512 + 160 * jj, odd = jj & 1;
            stride = F_;
            if (G == 2) { base = (ct < 2) ? wg : wu; col = f0 + (odd ? 0 : 128) + (ct & 1) * 16; }
            else { base = (G < 2) ? wg : wu; const int gi = (G < 2) ? G : G - 3; col = f0 + (odd ? 32 : 0) + gi * 64 + ct * 16; }
        }
        gB[i] = (const char*)(base + (size_t)k * stride + col + half * 8);
        incB[i] = 32u * stride * 2u;
    }
    const unsigned long long splitMask = (w & 1) ? 0xFFFFFFFF00000000ull : 0x00000000FFFFFFFFull;
    const unsigned dA = lbase + L_A + 8 * w * 128, dH = lbase + L_A + 2 * w * 128;
    const unsigned dB = lbase + L_B + w * 1024, dB2 = lbase + L_B + (16 + (w >> 1)) * 1024;
    auto issueA = [&](auto I, int S, unsigned slot) {
        constexpr int i = I;
        if (i == 0) glds_s<16 * 128>(xb + S * 128, voffA, dA + slot);
        if (i == 1) glds_s<80 * 128>(xb + 65536 + S * 128, voffA, dA + slot);
        if (i == 2 && TA) glds_sm16<0>((const char*)x + S * 128, voffH, dH + slot);
    };
    auto issueB = [&](auto I, unsigned slot) {
        constexpr int i = I;
        if (i == 0) glds_v<0>(gB[0], dB + slot);
        if (i == 1) glds_v<8192>(gB[1], dB + slot);
        if (i == 2) glds_vm<0>(gB[2], dB2 + slot, splitMask);
        gB[i] += incB[i];
    };
    const int g = l >> 4, tok = l & 15;
    unsigned adA[2];
    {
        const int sw = tok >> 1;
        adA[0] = lbase + L_A + (64 * m + tok) * 128 + ((g ^ sw) * 16);
        adA[1] = lbase + L_A + (64 * m + tok) * 128 + (((4 + g) ^ sw) * 16);
    }
    unsigned adB0, adB1, adB2;
    {
        const int b = g & 1, q = tok >> 2, pp = l & 3;
        const int f2 = (q >> 1) | (b << 1);
        const unsigned com = lbase + L_B + (8 * g + q) * 128;
        if (TA) { adB0 = com + ((j ^ f2) * 32) + pp * 8; adB1 = 0; adB2 = 0; }
        else {
            adB0 = com + (j >> 1) * 4096 + (((2 * (j & 1)) ^ f2) * 32) + pp * 8;
            adB2 = adB0 ^ 32;
            adB1 = com + 2 * 4096 + ((((pp >> 1) * 2 + (j >> 1)) ^ f2) * 32) + (j & 1) * 16 + (pp & 1) * 8;
        }
    }
    const unsigned adS = lbase + L_A + (2 + w) * 1024 + l * 16;
    const unsigned adSH = lbase + L_A + (w & 1) * 1024 + l * 16;

    f32x4 acc[4][5];
    f32x4 acch[2];
#pragma unroll
    for (int i = 0; i < 4; ++i)
#pragma unroll
        for (int c = 0; c < 5; ++c) acc[i][c] = f32x4{0, 0, 0, 0};
    acch[0] = acch[1] = f32x4{0, 0, 0, 0};
    float rs[3] = {0, 0, 0}, rq[3] = {0, 0, 0}, cs[5] = {0, 0, 0, 0, 0};
    bf16x8 fa[2][5], fb[2][5], sv[3];

    auto rdfrag = [&](auto SET, auto R, unsigned a_, unsigned b0, unsigned b1, unsigned b2) {
        constexpr int set = SET, r = R;
        if constexpr (r < 5) {
            if constexpr (TA) fb[set][r] = lds_b(b0 + r * 4096);
            else fb[set][r] = (r == 0) ? lds_b(b0) : (r == 1) ? lds_b(b2) : (r == 2) ? lds_b(b0 + 3 * 4096) : (r == 3) ? lds_b(b2 + 3 * 4096) : lds_b(b1);
        } else {
            constexpr int i = (r == 9) ? 0 : r - 4;
            if constexpr (TA || i > 0) fa[set][i] = lds_a(a_ + i * 2048);
        }
    };
    auto mf = [&](auto SET, auto I) {
        constexpr int set = SET, idx = I;
        if constexpr (idx < 20) {
            constexpr int c = idx >> 2, i = idx & 3;
            if constexpr (TA && c == 2) acc[i][c] = mfma32(fa[set][i + 1], fb[set][c], acc[i][c]);
            else acc[i][c] = mfma32(fb[set][c], fa[set][i + 1], acc[i][c]);
        } else if constexpr (TA) {
            if constexpr (idx == 20) acch[0] = mfma32(fb[set][1], fa[set][0], acch[0]);
            else acch[1] = mfma32(fa[set][0], fb[set][2], acch[1]);
        }
    };

    sfor<3>([&](auto I) { issueA(I, 0, 0); });
    sfor<3>([&](auto I) { issueB(I, 0); });
    sfor<3>([&](auto I) { issueB(I, BBLK); });
    sfor<3>([&](auto I) { issueA(I, 1, ABLK); });
    sfor<3>([&](auto I) { issueB(I, 2 * BBLK); });
    sfor<3>([&](auto I) { issueB(I, 3 * BBLK); });
    sfor<3>([&](auto I) { issueA(I, 2, 2 * ABLK); });
    sfor<3>([&](auto I) { issueB(I, 4 * BBLK); });
    wait_bar<2 * NAI + 12>();
    sfor<10>([&](auto R) { rdfrag(std::integral_constant<int, 0>{}, R, adA[0], adB0, adB1, adB2); });
    unsigned bo = 0, ao = 0;
    typedef std::integral_constant<int, 0> I0;
    typedef std::integral_constant<int, 1> I1;
#pragma unroll 1
    for (int s = 0; s < 16; s += 2) {
        {
            if (s >= 12) asm volatile("s_waitcnt vmcnt(0)" ::: "memory");
            wait_bar<2 * NAI + 9>();
            const unsigned bn = (bo == (NB - 1) * BBLK) ? 0u : bo + BBLK;
            const unsigned a_ = adA[1] + ao, b0 = adB0 + bn, b1 = adB1 + bn, b2 = adB2 + bn;
            const bool iss = s < 11;
            sv[0] = lds_a(adS + ao); sv[1] = lds_a(adS + ao + 8192); if (TA) sv[2] = lds_a(adSH + ao);
            SB();
            sfor<22>([&](auto I) {
                constexpr int idx = I;
                mf(I0{}, I);
                if constexpr (idx < 10) rdfrag(I1{}, I, a_, b0, b1, b2);
                if constexpr (idx < 20) cs[idx >> 2] = dot1(word(fb[0][idx >> 2], idx & 3), cs[idx >> 2]);
                if constexpr (idx >= 8 && idx < 20) {
                    constexpr int t = idx - 8, v = t >> 2, e = t & 3;
                    if constexpr (TA || v < 2) { rs[v] = dot1(word(sv[v], e), rs[v]); rq[v] = dots(word(sv[v], e), rq[v]); }
                }
                if constexpr (idx == 2 || idx == 8 || idx == 14) { if (iss) issueB(std::integral_constant<int, (idx - 2) / 6>{}, bo); }
                SB();
            });
            bo = bn;
        }
        {
            wait_bar<NAI + 9>();
            const unsigned bn = (bo == (NB - 1) * BBLK) ? 0u : bo + BBLK;
            const unsigned an = (ao == (NA - 1) * ABLK) ? 0u : ao + ABLK;
            const unsigned a_ = adA[0] + an, b0 = adB0 + bn, b1 = adB1 + bn, b2 = adB2 + bn;
            const bool iss = s < 10;
            SB();
            sfor<22>([&](auto I) {
                constexpr int idx = I;
                mf(I1{}, I);
                if constexpr (idx < 10) rdfrag(I0{}, I, a_, b0, b1, b2);
                if constexpr (idx < 20) cs[idx >> 2] = dot1(word(fb[1][idx >> 2], idx & 3), cs[idx >> 2]);
                if constexpr (idx == 1 || idx == 4 || idx == 7) { if (iss) issueA(std::integral_constant<int, (idx - 1) / 3>{}, s / 2 + 3, ao); }
                if constexpr (idx == 10 || idx == 13 || idx == 16) { if (iss) issueB(std::integral_constant<int, (idx - 10) / 3>{}, bo); }
                SB();
            });
            bo = bn; ao = an;
        }
    }
    {
#pragma unroll
        for (int i = 0; i < 3; ++i) {
            if (i == 2 && !TA) break;
#pragma unroll
            for (int o = 1; o < 8; o <<= 1) { rs[i] += __shfl_xor(rs[i], o); rq[i] += __shfl_xor(rq[i], o); }
            const float mean = rs[i] * (1.f / 512.f);
            float var = rq[i] * (1.f / 512.f) - mean * mean; var = var < 0.f ? 0.f : var;
            const float sd = rbf(sqrtf(rbf(rbf(var) + 1e-5f)));
            const float rstd = 1.f / sd;
            const int ir = (i == 2 ? 8 * (w & 1) : 16 + 64 * i + 8 * w) + (l >> 3);
            if ((l & 7) == 0 && (i < 2 || w < 2)) *(LAS f32x2*)(uintptr_t)(lbase + L_ROW + ir * 8) = f32x2{-rbf(mean) * rstd, rstd};
        }
#pragma unroll
        for (int c = 0; c < 5; ++c) {
            cs[c] = xadd32(xadd16(cs[c]));
            if (l < 16) *(LAS float*)(uintptr_t)(lbase + L_COL + (w * 80 + c * 16 + l) * 4) = cs[c];
        }
    }
    bar_lgkm();
    f32x2 st[5];
#pragma unroll
    for (int i = TA ? 0 : 1; i < 5; ++i) st[i] = *(const LAS f32x2*)(uintptr_t)(lbase + L_ROW + (64 * m + 16 * i + tok) * 8);
    f32x4 cv[5];
#pragma unroll
    for (int c = 0; c < 5; ++c) cv[c] = *(const LAS f32x4*)(uintptr_t)(lbase + L_COL + (w * 80 + c * 16 + 4 * g) * 4);
    constexpr int OST = TA ? 272 : 336;
    const unsigned ob = lbase + L_B;
    auto fix = [&](f32x4 a, f32x2 s, f32x4 c) { return a * s[1] + c * s[0]; };
    auto hmul = [&](f32x4 ga, f32x4 ua) { u32x2 r;
        r[0] = pk2(fmaxf(ga[0], 0.f) * ua[0], fmaxf(ga[1], 0.f) * ua[1]);
        r[1] = pk2(fmaxf(ga[2], 0.f) * ua[2], fmaxf(ga[3], 0.f) * ua[3]);
        return r; };
    if (TA) {
#pragma unroll
        for (int i = 0; i < 4; ++i) {
            f32x4 ga = fix(acc[i][3], st[i + 1], cv[3]), ua = fix(acc[i][4], st[i + 1], cv[4]);
            *(LAS u32x2*)(uintptr_t)(ob + (64 * m + 16 * i + tok) * OST + 128 + j * 32 + g * 8) = hmul(ga, ua);
        }
        const float csv = cs[2];
        s16x4 kf[5]; u32x2 vf[5];
#pragma unroll
        for (int i = 0; i < 5; ++i) {
            f32x4 ka = fix(i == 0 ? acch[0] : acc[i == 0 ? 0 : i - 1][1], st[i], cv[1]);
            u32x2 kp; kp[0] = pk2(ka[0], ka[1]); kp[1] = pk2(ka[2], ka[3]);
            kf[i] = __builtin_bit_cast(s16x4, kp);
            f32x4 va = (i == 0) ? acch[1] : acc[i == 0 ? 0 : i - 1][2];
            f32x4 s01 = *(const LAS f32x4*)(uintptr_t)(lbase + L_ROW + (64 * m + 16 * i + 4 * g) * 8);
            f32x4 s23 = *(const LAS f32x4*)(uintptr_t)(lbase + L_ROW + (64 * m + 16 * i + 4 * g + 2) * 8);
            vf[i][0] = pk2(va[0] * s01[1] + s01[0] * csv, va[1] * s01[3] + s01[2] * csv);
            vf[i][1] = pk2(va[2] * s23[1] + s23[0] * csv, va[3] * s23[3] + s23[2] * csv);
        }
        const bool par = tok & 1;
        const int da = tok - (4 * g + (tok & 1)), db = da - 2;
        const bool ca = da >= 0, cb = db >= 0;
#pragma unroll
        for (int i = 0; i < 4; ++i) {
            f32x4 qa = fix(acc[i][0], st[i + 1], cv[0]);
            u32x2 qp; qp[0] = pk2(qa[0], qa[1]); qp[1] = pk2(qa[2], qa[3]);
            s16x4 qf = __builtin_bit_cast(s16x4, qp);
            f32x4 z = {0, 0, 0, 0};
            f32x4 sp = __builtin_amdgcn_mfma_f32_16x16x16bf16_1k(kf[i], qf, z, 0, 0, 0);
            f32x4 sc = __builtin_amdgcn_mfma_f32_16x16x16bf16_1k(kf[i + 1], qf, z, 0, 0, 0);
            const bool first = (r0 + 64 * m + 16 * i) == 0;
            float la = ca ? (par ? sc[1] : sc[0]) : (par ? sp[1] : sp[0]);
            float lb = cb ? (par ? sc[3] : sc[2]) : (par ? sp[3] : sp[2]);
            la = rbf(la) * 0.25f; lb = rbf(lb) * 0.25f;
            if (i == 0) { if (first) { la = ca ? la : -3.0e38f; lb = cb ? lb : -3.0e38f; } }
            const float mx = xmax32(xmax16(fmaxf(la, lb)));
            const float ea = __expf(la - mx), eb = __expf(lb - mx);
            const float sum = xadd32(xadd16(ea + eb));
            const float inv = 1.f / sum;
            const unsigned pw = pk2(ea * inv, eb * inv);
            const unsigned wa = par ? (pw << 16) : (pw & 0xffffu), wb = par ? (pw & 0xffff0000u) : (pw >> 16);
            u32x4 pp; pp[0] = ca ? 0u : wa; pp[1] = cb ? 0u : wb; pp[2] = ca ? wa : 0u; pp[3] = cb ? wb : 0u;
            u32x4 vv; vv[0] = vf[i][0]; vv[1] = vf[i][1]; vv[2] = vf[i + 1][0]; vv[3] = vf[i + 1][1];
            f32x4 o = mfma32(__builtin_bit_cast(bf16x8, vv), __builtin_bit_cast(bf16x8, pp), z);
            u32x2 op; op[0] = pk2(o[0], o[1]); op[1] = pk2(o[2], o[3]);
            *(LAS u32x2*)(uintptr_t)(ob + (64 * m + 16 * i + tok) * OST + j * 32 + g * 8) = op;
        }
        bar_lgkm();
#pragma unroll
        for (int it = 0; it < 4; ++it) {
            const int id = it * 512 + tid, row = id >> 4, ch = id & 15;
            u32x4 v = *(const LAS u32x4*)(uintptr_t)(ob + row * OST + ch * 16);
            const int col = (ch < 8) ? (64 * j0 + ch * 8) : (512 + 64 * j0 + (ch - 8) * 8);
            *(u32x4*)(a2 + (size_t)(r0 + row) * A2LD + col) = v;
        }
    } else {
        const int jj = j0 - 8, odd = jj & 1;
        const int lo_off = (odd ? 64 : 0) + j * 64;
        const int mx_off = (odd ? 0 : 256) + j * 16;
#pragma unroll
        for (int i = 0; i < 4; ++i) {
#pragma unroll
            for (int c = 0; c < 2; ++c) {
                f32x4 ga = fix(acc[i][c], st[i + 1], cv[c]), ua = fix(acc[i][c + 2], st[i + 1], cv[c + 2]);
                *(LAS u32x2*)(uintptr_t)(ob + (64 * m + 16 * i + tok) * OST + lo_off + c * 32 + g * 8) = hmul(ga, ua);
            }
        }
#pragma unroll
        for (int i = 0; i < 4; i += 2) {
            f32x4 xa = fix(acc[i][4], st[i + 1], cv[4]), ya = fix(acc[i + 1][4], st[i + 2], cv[4]);
            f32x4 ga, ua;
#pragma unroll
            for (int e = 0; e < 4; ++e) {
                auto r = __builtin_amdgcn_permlane32_swap(__float_as_uint(xa[e]), __float_as_uint(ya[e]), false, false);
                ga[e] = __uint_as_float(r[0]); ua[e] = __uint_as_float(r[1]);
            }
            *(LAS u32x2*)(uintptr_t)(ob + (64 * m + 16 * (i + (g >> 1)) + tok) * OST + mx_off + (g & 1) * 8) = hmul(ga, ua);
        }
        bar_lgkm();
        const int f0 = 512 + 160 * jj;
#pragma unroll
        for (int it = 0; it < 5; ++it) {
            const int id = it * 512 + tid, row = id / 20, ch = id % 20;
            u32x4 v = *(const LAS u32x4*)(uintptr_t)(ob + row * OST + ch * 16);
            *(u32x4*)(a2 + (size_t)(r0 + row) * A2LD + 512 + f0 + ch * 8) = v;
        }
    }
}

__global__ __launch_bounds__(512, 2) void k1(const u16* __restrict__ x, const u16* __restrict__ wqkv, const u16* __restrict__ wg,
                                             const u16* __restrict__ wu, u16* __restrict__ a2) {
    extern __shared__ __attribute__((aligned(16))) char smem[];
    const unsigned lbase = (unsigned)(uintptr_t)smem;
    const int b = blockIdx.x, xcd = b & 7, loc = b >> 3;
    const int p = (xcd & 1) * 8 + (loc & 7), qd = xcd >> 1, c4 = loc >> 3;
    if (c4 < 2) k1_body<true>(x, wqkv, wg, wu, a2, p, 2 * qd + c4, lbase);
    else k1_body<false>(x, wqkv, wg, wu, a2, p, 8 + 2 * qd + (c4 - 2), lbase);
}

constexpr int NS = 5, SBLK = 32768, NST = 18;
constexpr int K2_LDS = NS * SBLK;

__global__ __launch_bounds__(512, 2) void k2(const u16* __restrict__ x, const u16* __restrict__ wo, const u16* __restrict__ wd,
                                             const u16* __restrict__ a2, u16* __restrict__ out) {
    extern __shared__ __attribute__((aligned(16))) char smem[];
    const unsigned lbase = (unsigned)(uintptr_t)smem;
    const int tid = threadIdx.x, l = tid & 63;
    const int w = __builtin_amdgcn_readfirstlane(tid >> 6);
    const int b = blockIdx.x, xcd = b & 7, loc = b >> 3;
    const int rt = (xcd & 1) * 16 + (xcd >> 1) * 4 + (loc & 3), ct = loc >> 2;
    const int R0 = rt * 64, C0 = ct * 64;
    if (w >= 4) {
        const int lw = w - 4;
        unsigned voffA, voffB;
        {
            const int row = 8 * lw + (l >> 3), ch = (l & 7) ^ ((row >> 1) & 7);
            voffA = (unsigned)(row * (A2LD * 2) + ch * 16);
            const int k = 8 * lw + (l >> 3);
            const int lch = (l & 7) ^ ((((k >> 1) & 1) | (((k >> 3) & 1) << 1)) << 1);
            voffB = (unsigned)(k * 1024 + lch * 16);
        }
        const char* sa = (const char*)(a2 + (size_t)R0 * A2LD);
        const char* sb = (const char*)(wo + C0);
        const char* wdb = (const char*)(wd + C0);
        const unsigned d0 = lbase + lw * 1024;
        auto issue = [&](int st, unsigned slot) {
            const unsigned d = d0 + slot;
            glds_s<16384>(sb, voffB, d);
            glds_s<16384 + 4096>(sb + 32768, voffB, d);
            glds_s<16384 + 8192>(sb + 65536, voffB, d);
            glds_s<16384 + 12288>(sb + 98304, voffB, d);
            glds_s<0>(sa, voffA, d);
            glds_s<4096>(sa + 32 * A2LD * 2, voffA, d);
            glds_s<8192>(sa + 128, voffA, d);
            glds_s<12288>(sa + 32 * A2LD * 2 + 128, voffA, d);
            sa += 256;
            sb = (st == 3) ? wdb : sb + 131072;
        };
#pragma unroll
        for (int st = 0; st < NS - 1; ++st) issue(st, st * SBLK);
        unsigned slot = (NS - 1) * SBLK;
#pragma unroll 1
        for (int st = 0; st < NST; ++st) {
            if (st + NS - 2 < NST) asm volatile("s_waitcnt vmcnt(%0)\n\ts_barrier" :: "n"(8 * (NS - 2)) : "memory");
            else if (st + NS - 2 == NST) asm volatile("s_waitcnt vmcnt(%0)\n\ts_barrier" :: "n"(8 * (NS - 3)) : "memory");
            else if (st + NS - 2 == NST + 1) asm volatile("s_waitcnt vmcnt(%0)\n\ts_barrier" :: "n"(8 * (NS - 4)) : "memory");
            else asm volatile("s_waitcnt vmcnt(0)\n\ts_barrier" ::: "memory");
            if (st + NS - 1 < NST) issue(st + NS - 1, slot);
            slot = (slot == (NS - 1) * SBLK) ? 0u : slot + SBLK;
        }
        return;
    }
    const int wm = w >> 1, wn = w & 1;
    const int g = l >> 4, tok = l & 15;
    unsigned adA[2], adB[2];
    {
        const int sw = tok >> 1;
        adA[0] = lbase + (32 * wm + tok) * 128 + ((g ^ sw) * 16);
        adA[1] = lbase + (32 * wm + tok) * 128 + (((4 + g) ^ sw) * 16);
        const int bb = g & 1, q = tok >> 2, pp = l & 3;
        const int f2 = (q >> 1) | (bb << 1);
        adB[0] = lbase + 16384 + (8 * g + q) * 128 + (((2 * wn) ^ f2) * 32) + pp * 8;
        adB[1] = adB[0] ^ 32;
    }
    u32x2 xv[2][2];
#pragma unroll
    for (int i = 0; i < 2; ++i)
#pragma unroll
        for (int c = 0; c < 2; ++c) xv[i][c] = *(const u32x2*)(x + (size_t)(R0 + 32 * wm + 16 * i + tok) * D_ + C0 + 32 * wn + 16 * c + 4 * g);
    f32x4 acc[2][2], sacc[2][2];
#pragma unroll
    for (int i = 0; i < 2; ++i)
#pragma unroll
        for (int c = 0; c < 2; ++c) { acc[i][c] = f32x4{0, 0, 0, 0}; sacc[i][c] = f32x4{0, 0, 0, 0}; }
    unsigned so = 0;
#pragma unroll 1
    for (int st = 0; st < NST; ++st) {
        bar_lgkm();
        if (st == 4) {
#pragma unroll
            for (int i = 0; i < 2; ++i)
#pragma unroll
                for (int c = 0; c < 2; ++c) { sacc[i][c] = acc[i][c]; acc[i][c] = f32x4{0, 0, 0, 0}; }
        }
        bf16x8 fa[4][2], fb[4][2];
        const unsigned a0 = adA[0] + so, a1 = adA[1] + so, b0 = adB[0] + so, b1 = adB[1] + so;
#pragma unroll
        for (int ks = 0; ks < 4; ++ks) {
#pragma unroll
            for (int i = 0; i < 2; ++i) fa[ks][i] = lds_a(((ks & 1) ? a1 : a0) + (ks >> 1) * 8192 + i * 2048);
            fb[ks][0] = lds_b(b0 + ks * 4096); fb[ks][1] = lds_b(b1 + ks * 4096);
        }
#pragma unroll
        for (int ks = 0; ks < 4; ++ks)
#pragma unroll
            for (int i = 0; i < 2; ++i)
#pragma unroll
                for (int c = 0; c < 2; ++c) acc[i][c] = mfma32(fb[ks][c], fa[ks][i], acc[i][c]);
        so = (so == (NS - 1) * SBLK) ? 0u : so + SBLK;
    }
#pragma unroll
    for (int i = 0; i < 2; ++i)
#pragma unroll
        for (int c = 0; c < 2; ++c) {
            float r[4];
#pragma unroll
            for (int e = 0; e < 4; ++e) {
                const float d = rbf(acc[i][c][e]);
                const float sg = rbf(d / (1.f + __expf(-d)));
                const float t = rbf(rbf(sacc[i][c][e]) * sg);
                const float xx = (e & 1) ? bfhi(xv[i][c][e >> 1]) : bflo(xv[i][c][e >> 1]);
                r[e] = xx + t;
            }
            u32x2 ov; ov[0] = pk2(r[0], r[1]); ov[1] = pk2(r[2], r[3]);
            *(u32x2*)(out + (size_t)(R0 + 32 * wm + 16 * i + tok) * D_ + C0 + 32 * wn + 16 * c + 4 * g) = ov;
        }
}

extern "C" void kernel_launch(void* const* d_in, const int* in_sizes, int n_in,
                              void* d_out, int out_size, void* d_ws, size_t ws_size,
                              hipStream_t stream) {
    const u16* x = (const u16*)d_in[0];
    const u16* wqkv = (const u16*)d_in[1];
    const u16* wo = (const u16*)d_in[2];
    const u16* wg = (const u16*)d_in[4];
    const u16* wu = (const u16*)d_in[5];
    const u16* wd = (const u16*)d_in[6];
    u16* out = (u16*)d_out;
    u16* a2 = (u16*)d_ws;
    (void)hipFuncSetAttribute((const void*)k1, hipFuncAttributeMaxDynamicSharedMemorySize, K1_LDS);
    (void)hipFuncSetAttribute((const void*)k2, hipFuncAttributeMaxDynamicSharedMemorySize, K2_LDS);
    k1<<<256, 512, K1_LDS, stream>>>(x, wqkv, wg, wu, a2);
    k2<<<256, 512, K2_LDS, stream>>>(x, wo, wd, a2, out);
}
```

```cpp
#include <hip/hip_runtime.h>
#include <stdint.h>
#include <utility>

typedef unsigned short u16;
typedef __attribute__((ext_vector_type(8))) __bf16 bf16x8;
typedef __attribute__((ext_vector_type(2))) __bf16 bf16x2;
typedef __attribute__((ext_vector_type(4))) short s16x4;
typedef __attribute__((ext_vector_type(8))) short s16x8;
typedef __attribute__((ext_vector_type(4))) float f32x4;
typedef __attribute__((ext_vector_type(2))) float f32x2;
typedef __attribute__((ext_vector_type(4))) unsigned u32x4;
typedef __attribute__((ext_vector_type(2))) unsigned u32x2;
#define LAS __attribute__((address_space(3)))
#define SB() __builtin_amdgcn_sched_barrier(0)
#define IC(n) std::integral_constant<int, (n)>{}
#define LDS(T, a) (*(LAS T*)(uintptr_t)(a))

constexpr int S_ = 2048, D_ = 512, F_ = 1792;
constexpr int A2LD = 2304;
constexpr float LOG2E = 1.4426950408889634f;

template <int... I, class Fn> __device__ __forceinline__ void sfor_impl(std::integer_sequence<int, I...>, Fn&& f) { (f(std::integral_constant<int, I>{}), ...); }
template <int N, class Fn> __device__ __forceinline__ void sfor(Fn&& f) { sfor_impl(std::make_integer_sequence<int, N>{}, f); }

__device__ __forceinline__ float rbf(float f) { return (float)(__bf16)f; }
__device__ __forceinline__ unsigned pk2(float lo, float hi) {
    bf16x2 v; v[0] = (__bf16)lo; v[1] = (__bf16)hi; return __builtin_bit_cast(unsigned, v);
}
__device__ __forceinline__ float bflo(unsigned u) { return __uint_as_float(u << 16); }
__device__ __forceinline__ float bfhi(unsigned u) { return __uint_as_float(u & 0xffff0000u); }

template <int OFF> __device__ __forceinline__ void setm0(unsigned base) {
    asm volatile("s_add_u32 m0, %0, %1" :: "s"(base), "n"(OFF) : "scc");
}
__device__ __forceinline__ void dma_v(const void* g) { asm volatile("global_load_lds_dwordx4 %0, off" :: "v"(g) : "memory"); }
__device__ __forceinline__ void dma_vm(const void* g, unsigned long long mask) {
    asm volatile("s_mov_b64 exec, %1\n\tglobal_load_lds_dwordx4 %0, off\n\ts_mov_b64 exec, -1" :: "v"(g), "s"(mask) : "memory");
}
template <int IO> __device__ __forceinline__ void dma_s(const void* sb, unsigned vo) {
    asm volatile("global_load_lds_dwordx4 %0, %1 offset:%2" :: "v"(vo), "s"(sb), "n"(IO) : "memory");
}
template <int IO> __device__ __forceinline__ void dma_s_sc1(const void* sb, unsigned vo) {
    asm volatile("global_load_lds_dwordx4 %0, %1 offset:%2 sc1" :: "v"(vo), "s"(sb), "n"(IO) : "memory");
}
template <int LIM> __device__ __forceinline__ void dma_s_c(int cnt, const void* sb, unsigned vo) {
    asm volatile("s_cmp_ge_i32 %2, %3\n\ts_cbranch_scc1 1f\n\tglobal_load_lds_dwordx4 %0, %1\n1:" :: "v"(vo), "s"(sb), "s"(cnt), "n"(LIM) : "memory", "scc");
}
template <int IO> __device__ __forceinline__ void dma_s32(const void* sb, unsigned vo) {
    asm volatile("s_mov_b64 exec, 0xffffffff\n\tglobal_load_lds_dwordx4 %0, %1 offset:%2\n\ts_mov_b64 exec, -1" :: "v"(vo), "s"(sb), "n"(IO) : "memory");
}
template <int LIM> __device__ __forceinline__ void dma_s16_c(int cnt, const void* sb, unsigned vo) {
    asm volatile("s_cmp_ge_i32 %2, %3\n\ts_cbranch_scc1 1f\n\ts_mov_b64 exec, 0xffff\n\tglobal_load_lds_dwordx4 %0, %1\n\ts_mov_b64 exec, -1\n1:"
                 :: "v"(vo), "s"(sb), "s"(cnt), "n"(LIM) : "memory", "scc");
}
__device__ __forceinline__ void nop1() { asm volatile("s_nop 0"); }
__device__ __forceinline__ void touch_v(const void* gaddr, unsigned ldsaddr) {
    asm volatile("s_mov_b32 m0, %1\n\ts_nop 0\n\tglobal_load_lds_dword %0, off" :: "v"(gaddr), "s"(ldsaddr) : "memory");
}
__device__ __forceinline__ void touch_v_sc1(const void* gaddr, unsigned ldsaddr) {
    asm volatile("s_mov_b32 m0, %1\n\ts_nop 0\n\tglobal_load_lds_dword %0, off sc1" :: "v"(gaddr), "s"(ldsaddr) : "memory");
}
template <int N> __device__ __forceinline__ void wait_vm() { asm volatile("s_waitcnt vmcnt(%0)" :: "n"(N) : "memory"); }
template <int N> __device__ __forceinline__ void wait_bar() {
    __builtin_amdgcn_s_waitcnt(0xC07F);
    asm volatile("s_waitcnt vmcnt(%0)\n\ts_barrier" :: "n"(N) : "memory");
}
__device__ __forceinline__ void bar_lgkm() { __builtin_amdgcn_s_waitcnt(0xC07F); asm volatile("s_barrier" ::: "memory"); }
__device__ __forceinline__ void bar() { asm volatile("s_barrier" ::: "memory"); }
__device__ __forceinline__ void ld2_sc1(unsigned& a, const unsigned* pa, unsigned& b, const unsigned* pb) {
    asm volatile("global_load_dword %0, %2, off sc1\n\tglobal_load_dword %1, %3, off sc1\n\ts_waitcnt vmcnt(0)"
                 : "=&v"(a), "=&v"(b) : "v"(pa), "v"(pb) : "memory");
}
__device__ __forceinline__ void st_sc1(unsigned* p, unsigned v, unsigned long long mask) {
    asm volatile("s_mov_b64 exec, %2\n\ts_cbranch_execz 1f\n\tglobal_store_dword %0, %1, off sc1\n1:\n\ts_mov_b64 exec, -1"
                 :: "v"(p), "v"(v), "s"(mask) : "memory");
}

__device__ __forceinline__ bf16x8 lds_a(unsigned addr) { return LDS(const bf16x8, addr); }
__device__ __forceinline__ bf16x8 lds_b(unsigned addr) {
    s16x4 lo = __builtin_amdgcn_ds_read_tr16_b64_v4i16((LAS s16x4*)(uintptr_t)addr);
    s16x4 hi = __builtin_amdgcn_ds_read_tr16_b64_v4i16((LAS s16x4*)(uintptr_t)(addr + 512));
    s16x8 r = __builtin_shufflevector(lo, hi, 0, 1, 2, 3, 4, 5, 6, 7);
    return __builtin_bit_cast(bf16x8, r);
}
__device__ __forceinline__ f32x4 mfma32(bf16x8 a, bf16x8 b, f32x4 c) { return __builtin_amdgcn_mfma_f32_16x16x32_bf16(a, b, c, 0, 0, 0); }
__device__ __forceinline__ void swap16(unsigned& a, unsigned& b) { auto r = __builtin_amdgcn_permlane16_swap(a, b, false, false); a = r[0]; b = r[1]; }
__device__ __forceinline__ void swap32(unsigned& a, unsigned& b) { auto r = __builtin_amdgcn_permlane32_swap(a, b, false, false); a = r[0]; b = r[1]; }
__device__ __forceinline__ void tr4(unsigned (&v)[4]) { swap16(v[0], v[1]); swap16(v[2], v[3]); swap32(v[0], v[2]); swap32(v[1], v[3]); }
__device__ __forceinline__ float xadd16(float v) { unsigned a = __float_as_uint(v), b = a; swap16(a, b); return __uint_as_float(a) + __uint_as_float(b); }
template <int CTRL> __device__ __forceinline__ float dadd(float v) {
    return v + __int_as_float(__builtin_amdgcn_update_dpp(0, __float_as_int(v), CTRL, 0xf, 0xf, true));
}
__device__ __forceinline__ float red8(float v) { return dadd<0x141>(dadd<0x4E>(dadd<0xB1>(v))); }
__device__ __forceinline__ f32x2 mkstat(float s, float q) {
    const float mean = s * (1.f / 512.f);
    float var = fmaxf(q * (1.f / 512.f) - mean * mean, 0.f);
    const float sd = rbf(__builtin_amdgcn_sqrtf(rbf(rbf(var) + 1e-5f)));
    return f32x2{-rbf(mean), __builtin_amdgcn_rcpf(sd)};
}

constexpr int NB = 5, NA = 3;
constexpr int BBLK = 20480, ABLK = 18432;
constexpr int L_B = 0, L_A = NB * BBLK, L_ST = L_A + NA * ABLK;
constexpr int L_MEAN = L_ST, L_RSTD = L_ST + 144 * 4;
constexpr int L_COL = L_ST + 144 * 8;
constexpr int L_DUMP = L_COL + 8 * 80 * 4;
constexpr int L_FE = L_DUMP + 256, L_FP = L_FE + 256;
constexpr int K_LDS = L_FP + 256;
constexpr unsigned SPIN_CAP = 1u << 16;
#define TOUCH_AT 2

template <bool TA>
__device__ __forceinline__ void p1_helper(const u16* x, const u16* wqkv, const u16* wo, const u16* wg, const u16* wu, const u16* wd,
                                          unsigned* myP, const unsigned* myE, const unsigned* pollP, unsigned* echoE,
                                          int p, int j0, int C0, unsigned& myv, unsigned& last) {
    const int l = threadIdx.x & 63;
    const int h = __builtin_amdgcn_readfirstlane(threadIdx.x >> 6) - 4;
    const int r0 = p * 128, kq = l >> 3;
    constexpr int H = TA ? 1 : 0;
    const char* xs = (const char*)x + (size_t)r0 * 1024;
    unsigned vA[4], vH = 0;
    {
        const int rr = 8 * h + kq;
        vA[0] = (unsigned)(rr * 1024 + (((l & 7) ^ ((rr >> 1) & 7)) * 16));
        vA[1] = vA[0] + 32768; vA[2] = vA[0] + 65536; vA[3] = vA[0] + 98304;
        if (TA) {
            int hr = 4 * h + (kq & 3); hr = hr < 2 ? hr + 2 : hr;
            int gr = hr - 16; gr = (r0 + gr < 0) ? -r0 : gr;
            const int pos = 4 * h + (kq & 3);
            (void)pos;
            vH = (unsigned)((gr + 16) * 1024 + (((l & 7) ^ ((hr >> 1) & 7)) * 16));
        }
    }
    const unsigned dA = L_A + 2048 + h * 1024;
    const unsigned dH = L_A + (h ? h * 512 : 256);
    auto issA = [&](auto HALF, unsigned slot, const char* xk) {
        constexpr int hf = HALF;
        setm0<hf * 8192>(dA + slot); nop1(); dma_s<0>(xk, vA[2 * hf]);
        setm0<hf * 8192 + 4096>(dA + slot); nop1(); dma_s<0>(xk, vA[2 * hf + 1]);
        if constexpr (TA && hf == 0) {
            setm0<0>(dH + slot);
            if (h == 0) dma_s16_c<1>(0, xk - 16384, vH); else dma_s32<0>(xk - 16384, vH);
        }
    };
    issA(IC(0), 0, xs); issA(IC(1), 0, xs);
    const char* sB[5]; unsigned vB0, vB1; const char* gM = nullptr;
    {
        const int k = 8 * h + kq;
        const int lch = (l & 7) ^ ((((k >> 1) & 1) | ((h & 1) << 1)) << 1);
        vB1 = (unsigned)(k * (F_ * 2) + lch * 16);
        if (TA) {
            vB0 = (unsigned)(k * 3072 + lch * 16);
            sB[0] = (const char*)(wqkv + 64 * j0); sB[1] = sB[0] + 1024; sB[2] = sB[0] + 2048;
            sB[3] = (const char*)(wg + 64 * j0); sB[4] = (const char*)(wu + 64 * j0);
        } else {
            const int jj = j0 - 8, f0 = 512 + 160 * jj, odd = jj & 1;
            vB0 = vB1;
            sB[0] = (const char*)(wg + f0 + (odd ? 32 : 0)); sB[1] = sB[0] + 128;
            sB[3] = (const char*)(wu + f0 + (odd ? 32 : 0)); sB[4] = sB[3] + 128;
            sB[2] = nullptr;
            gM = (const char*)(((lch < 4) ? wg : wu) + (size_t)k * F_ + f0 + (odd ? 0 : 128) + (lch & 3) * 8);
        }
    }
    const unsigned dB = L_B + h * 1024;
    auto issB = [&](unsigned slot) {
        setm0<0>(dB + slot); nop1(); dma_s<0>(sB[0], vB0);
        setm0<4096>(dB + slot); nop1(); dma_s<0>(sB[1], vB0);
        setm0<8192>(dB + slot); nop1(); if (TA) dma_s<0>(sB[2], vB0); else dma_v(gM);
        setm0<12288>(dB + slot); nop1(); dma_s<0>(sB[3], vB1);
        setm0<16384>(dB + slot); nop1(); dma_s<0>(sB[4], vB1);
        vB1 += 32 * F_ * 2;
        if (TA) vB0 += 32 * 3072; else { vB0 = vB1; gM += 32 * F_ * 2; }
    };
    issB(0); issB(BBLK);
    unsigned vW;
    { const int k = 8 * h + kq; vW = (unsigned)(k * 1024 + (((l & 7) ^ ((((k >> 1) & 1) | ((h & 1) << 1)) << 1)) * 16)); }
    auto issW = [&](const char* sw, unsigned slot) {
        setm0<0>(dB + slot); nop1(); dma_s<0>(sw, vW);
        setm0<4096>(dB + slot); nop1(); dma_s<0>(sw + 32768, vW);
        setm0<8192>(dB + slot); nop1(); dma_s<0>(sw + 65536, vW);
        setm0<12288>(dB + slot); nop1(); dma_s<0>(sw + 98304, vW);
    };
    if (TA) { if (l < 24 && h == 1) LDS(u32x4, L_A + (l >> 3) * ABLK + (l & 7) * 16) = u32x4{0x3f803f80u, 0x3f803f80u, 0x3f803f80u, 0x3f803f80u}; }
    f32x2 rs[5], rq[5];
#pragma unroll
    for (int i = 0; i < 5; ++i) rs[i] = rq[i] = f32x2{0, 0};
    auto st2 = [&](unsigned u, f32x2& s_, f32x2& q_) {
        float lo, hi, s0_ = s_[0], s1_ = s_[1], q0_ = q_[0], q1_ = q_[1];
        asm volatile("v_lshlrev_b32 %0, 16, %6\n\tv_and_b32 %1, 0xffff0000, %6\n\tv_add_f32 %2, %2, %0\n\tv_add_f32 %3, %3, %1\n\t"
                     "v_fmac_f32 %4, %0, %0\n\tv_fmac_f32 %5, %1, %1"
                     : "=&v"(lo), "=&v"(hi), "+v"(s0_), "+v"(s1_), "+v"(q0_), "+v"(q1_) : "v"(u));
        s_ = f32x2{s0_, s1_}; q_ = f32x2{q0_, q1_};
    };
    const unsigned adS = L_A + 2048 + h * 1024 + l * 16;
    const unsigned adSH = L_A + h * 512 + l * 8;
    wait_bar<5>();
    issA(IC(0), ABLK, xs + 128); issA(IC(1), ABLK, xs + 128);
    issB(2 * BBLK);
    unsigned bs = 3 * BBLK, as = 2 * ABLK, ar = 0;
    xs += 256;
    const char* sw = (const char*)(wo + C0);
    const unsigned special = (1u << TOUCH_AT) | (h == 0 ? 0x1111u : 0u);
    auto interval = [&](auto ODD, auto MODE) {
        constexpr int od = ODD, mode = MODE;
        const unsigned sa_ = adS + ar;
        const u32x4 s0 = LDS(const u32x4, sa_ + od * 8192), s1 = LDS(const u32x4, sa_ + od * 8192 + 4096);
        u32x2 sh = {0, 0};
        if constexpr (TA && od == 0) sh = LDS(const u32x2, adSH + ar);
        const unsigned mb = dB + bs;
        SB();
        if constexpr (mode == 0) issA(ODD, as, xs);
        SB();
        sfor<5>([&](auto I) {
            constexpr int i = I;
            if constexpr (mode < 2 || i < 4) setm0<i * 4096>(mb);
            if constexpr (i < 4) { const unsigned u = s0[i], v = s1[i]; st2(u, rs[2 * od], rq[2 * od]); st2(v, rs[2 * od + 1], rq[2 * od + 1]); }
            else if constexpr (TA && od == 0) { const unsigned u = sh[0], v = sh[1]; st2(u, rs[4], rq[4]); st2(v, rs[4], rq[4]); }
            else nop1();
            SB();
            if constexpr (mode < 2) {
                if constexpr (i == 2 && !TA) dma_v(gM); else dma_s<0>(sB[i], i < 3 ? vB0 : vB1);
            } else if constexpr (i < 4) dma_s<0>(sw, vW + i * 32768);
        });
        if constexpr (mode < 2) {
            vB1 += 32 * F_ * 2;
            if (TA) vB0 += 32 * 3072; else { vB0 = vB1; gM += 32 * F_ * 2; }
        } else sw += 131072;
        bs = (bs == (NB - 1) * BBLK) ? 0u : bs + BBLK;
        if constexpr (od == 1) { as = ar; ar = (ar == (NA - 1) * ABLK) ? 0u : ar + ABLK; xs += 128; }
    };
#pragma unroll 1
    for (int t = 0; t < 12; t += 2) {
        interval(IC(0), IC(0));
        if ((special >> t) & 1) {
            if (h == 0) {
                if (t == 0) {
                    const unsigned ev = LDS(const volatile unsigned, L_FE + l * 4);
                    unsigned v = 0;
                    while (__ballot(ev == v) != 0ull) v += 2;
                    myv = v;
                    st_sc1(myP, v, 1ull);
                } else if (t == 4) touch_v_sc1(pollP, L_FP);
                else if (t == 8) {
                    const unsigned pv = LDS(const volatile unsigned, L_FP + l * 4);
                    st_sc1(echoE, pv, __ballot(!(pv & 1)) & 0xffffull);
                    last = (pv & 1) ? last : pv;
                }
            }
            if (t == TOUCH_AT) {
#pragma unroll
                for (int i = 0; i < 3; ++i) {
                    const int idx = (blockIdx.x >> 3) * 9 + h + 4 * i;
                    if (h + 4 * i < 9) touch_v((idx < 64 ? (const char*)wo + idx * 8192 : (const char*)wd + (idx - 64) * 8192) + l * 128, L_DUMP);
                }
            }
        }
        wait_bar<14 + H>();
        interval(IC(1), IC(0));
        wait_bar<14 + H>();
    }
    interval(IC(0), IC(1));
    if (h == 0) touch_v_sc1(myE, L_FE);
    wait_bar<12>();
    interval(IC(1), IC(2));
    wait_bar<9>();
    interval(IC(0), IC(2));
    wait_bar<8>();
    interval(IC(1), IC(2));
    {
#pragma unroll
        for (int i = 0; i < 4; ++i) {
            const f32x2 st_ = mkstat(red8(rs[i][0] + rs[i][1]), red8(rq[i][0] + rq[i][1]));
            const int ir = 16 + 64 * (i >> 1) + 32 * (i & 1) + 8 * h + kq;
            if ((l & 7) == 0) { LDS(float, L_MEAN + ir * 4) = st_[0]; LDS(float, L_RSTD + ir * 4) = st_[1]; }
        }
        if (TA) {
            const f32x2 st_ = mkstat(dadd<0x140>(red8(rs[4][0] + rs[4][1])), dadd<0x140>(red8(rq[4][0] + rq[4][1])));
            if ((l & 15) == 0) { LDS(float, L_MEAN + (4 * h + (l >> 4)) * 4) = st_[0]; LDS(float, L_RSTD + (4 * h + (l >> 4)) * 4) = st_[1]; }
        }
    }
    bar_lgkm();
    issW((const char*)(wo + C0) + 3 * 131072, 4 * BBLK);
    issW((const char*)(wd + C0), 0);
}

template <bool TA>
__device__ __forceinline__ void p1_mfma(int p, int j0) {
    const int l = threadIdx.x & 63;
    const int j = __builtin_amdgcn_readfirstlane(threadIdx.x >> 6);
    const int g = l >> 4, tok = l & 15;
    const unsigned adA0 = L_A + tok * 128 + ((g ^ (tok >> 1)) * 16);
    const int f2 = ((tok >> 3) & 1) | ((g & 1) << 1);
    const unsigned comB = L_B + (8 * g + (tok >> 2)) * 128;
    unsigned adB0, adB1 = 0;
    {
        const int pp = l & 3;
        if (TA) adB0 = comB + ((j ^ f2) * 32) + pp * 8;
        else {
            adB0 = comB + (j >> 1) * 4096 + (((2 * (j & 1)) ^ f2) * 32) + pp * 8;
            adB1 = comB + 2 * 4096 + ((((pp >> 1) * 2 + (j >> 1)) ^ f2) * 32) + (j & 1) * 16 + (pp & 1) * 8;
        }
    }
    f32x4 acc[8][5], acch[2], accc[5];
#pragma unroll
    for (int i = 0; i < 8; ++i)
#pragma unroll
        for (int c = 0; c < 5; ++c) acc[i][c] = f32x4{0, 0, 0, 0};
    acch[0] = acch[1] = f32x4{0, 0, 0, 0};
#pragma unroll
    for (int c = 0; c < 5; ++c) accc[c] = f32x4{0, 0, 0, 0};
    bf16x8 fa[6], fb[5];
    bf16x8 ones = __builtin_bit_cast(bf16x8, u32x4{0x3f803f80u, 0x3f803f80u, 0x3f803f80u, 0x3f803f80u});
    asm volatile("" : "+v"(ones));
    unsigned ra = adA0, rb0 = adB0, rb1 = adB1, rb2 = adB0 ^ 32;
    unsigned bo = 0, ao = 0;
    auto rdB = [&](auto R) {
        constexpr int r = R;
        if constexpr (TA) fb[r] = lds_b(rb0 + r * 4096);
        else fb[r] = (r == 0) ? lds_b(rb0) : (r == 1) ? lds_b(rb2) : (r == 2) ? lds_b(rb0 + 3 * 4096) : (r == 3) ? lds_b(rb2 + 3 * 4096) : lds_b(rb1);
    };
    auto mm = [&](auto I, auto C, auto SL) {
        constexpr int i = I, c = C, sl = SL;
        if constexpr (TA && c == 2) acc[i][c] = mfma32(fa[sl], fb[c], acc[i][c]);
        else acc[i][c] = mfma32(fb[c], fa[sl], acc[i][c]);
    };
    auto step = [&](auto PAR) {
        constexpr int par = PAR, X = par ? 3 : 0, Y = par ? 0 : 3;
        sfor<15>([&](auto I) {
            constexpr int idx = I, c = idx / 3, i = idx % 3;
            mm(IC(i), IC(c), IC(X + i));
            if constexpr (idx == 0) fa[Y] = lds_a(ra + 4 * 2048);
            if constexpr (idx == 1) fa[Y + 1] = lds_a(ra + 5 * 2048);
            if constexpr (idx == 2 && TA) fa[Y + 2] = lds_a(ra);
            if constexpr (idx == 4) { bo = (bo == (NB - 1) * BBLK) ? 0u : bo + BBLK; if constexpr (par == 1) ao = (ao == (NA - 1) * ABLK) ? 0u : ao + ABLK; asm volatile("" : "+s"(bo), "+s"(ao)); }
            if constexpr (idx == 6) { rb0 = adB0 + bo; asm volatile("" : "+v"(rb0)); }
            if constexpr (idx == 8 && !TA) { rb1 = adB1 + bo; asm volatile("" : "+v"(rb1)); }
            if constexpr (idx == 10 && !TA) { rb2 = rb0 ^ 32; asm volatile("" : "+v"(rb2)); }
            SB();
        });
        sfor<10>([&](auto I) {
            constexpr int idx = I, c = idx / 2, i = idx % 2;
            mm(IC(3 + i), IC(c), IC(Y + i));
            if constexpr (idx < 3) fa[X + idx] = lds_a(ra + (6 + idx) * 2048);
            if constexpr (idx == 4) { ra = (par == 0) ? (ra ^ 64) : adA0 + ao; asm volatile("" : "+v"(ra)); }
            SB();
        });
        if constexpr (TA) {
            acch[0] = mfma32(fb[1], fa[Y + 2], acch[0]);
            acch[1] = mfma32(fa[Y + 2], fb[2], acch[1]);
        }
        SB();
        bar_lgkm();
        SB();
        sfor<5>([&](auto C) {
            constexpr int c = C;
            mm(IC(5), C, IC(X));
            if constexpr (c < 3) fa[Y + c] = lds_a(ra + (1 + c) * 2048);
            SB();
            mm(IC(6), C, IC(X + 1));
            mm(IC(7), C, IC(X + 2));
            if constexpr (!TA || (c != 1 && c != 2)) accc[c] = mfma32(fb[c], ones, accc[c]);
            SB();
            rdB(C);
            SB();
        });
    };
    __builtin_amdgcn_s_setprio(1);
    SB();
    bar_lgkm();
    SB();
    sfor<5>([&](auto C) {
        constexpr int c = C;
        if constexpr (c < 3) fa[c] = lds_a(ra + (1 + c) * 2048);
        SB();
        rdB(C);
        SB();
    });
#pragma unroll 1
    for (int s = 0; s < 16; s += 2) {
        step(IC(0));
        step(IC(1));
    }
    __builtin_amdgcn_s_setprio(0);
    constexpr int OST = TA ? 272 : 336;
    const unsigned ow = L_A + tok * OST + g * 8;
    auto hmul = [&](f32x4 ga, f32x4 ua, float r2) {
        f32x4 t;
#pragma unroll
        for (int e = 0; e < 4; ++e) t[e] = fmaxf(ga[e], 0.f);
        t = t * ua * r2;
        return u32x2{pk2(t[0], t[1]), pk2(t[2], t[3])};
    };
    float nm[9], rsd[9];
#pragma unroll
    for (int i = TA ? 0 : 1; i < 9; ++i) { nm[i] = LDS(const float, L_MEAN + (16 * i + tok) * 4); rsd[i] = LDS(const float, L_RSTD + (16 * i + tok) * 4); }
    if (TA) {
#pragma unroll
        for (int t = 0; t < 8; ++t)
            LDS(u32x2, ow + 16 * t * OST + 128 + j * 32) = hmul(acc[t][3] + accc[3] * nm[t + 1], acc[t][4] + accc[4] * nm[t + 1], rsd[t + 1] * rsd[t + 1]);
        SB();
        f32x4 ck;
#pragma unroll
        for (int e = 0; e < 4; ++e) ck[e] = __int_as_float(__builtin_amdgcn_update_dpp(0, __float_as_int(acch[0][e]), 0x150, 0xf, 0xf, false));
        float csv;
        { unsigned a = __float_as_uint(acch[1][0]), b = a; swap16(a, b); unsigned c_ = a, d = a; swap32(c_, d); csv = __uint_as_float(c_); }
        const bool par = tok & 1;
        const int da = tok - (4 * g + (tok & 1));
        const bool ca = da >= 0, cb = da >= 2;
        const f32x4 z = {0, 0, 0, 0};
        s16x4 kprev; u32x2 vprev;
        {
            const f32x4 ka = (acch[0] + ck * nm[0]) * rsd[0];
            kprev = __builtin_bit_cast(s16x4, u32x2{pk2(ka[0], ka[1]), pk2(ka[2], ka[3])});
            const f32x4 vm = LDS(const f32x4, L_MEAN + 4 * g * 4), vr = LDS(const f32x4, L_RSTD + 4 * g * 4);
            const f32x4 va = (acch[1] + vm * csv) * vr;
            vprev = u32x2{pk2(va[0], va[1]), pk2(va[2], va[3])};
            if (g == 0) vprev[0] = 0u;
        }
#pragma unroll
        for (int hb = 0; hb < 2; ++hb) {
            s16x4 kf[5], qf[4]; u32x2 vf[5];
            f32x4 vm[4], vr[4];
#pragma unroll
            for (int i = 0; i < 4; ++i) { vm[i] = LDS(const f32x4, L_MEAN + (16 * (4 * hb + i + 1) + 4 * g) * 4); vr[i] = LDS(const f32x4, L_RSTD + (16 * (4 * hb + i + 1) + 4 * g) * 4); }
            kf[0] = kprev; vf[0] = vprev;
#pragma unroll
            for (int i = 0; i < 4; ++i) {
                const int t = 4 * hb + i;
                const f32x4 ka = (acc[t][1] + ck * nm[t + 1]) * rsd[t + 1];
                kf[i + 1] = __builtin_bit_cast(s16x4, u32x2{pk2(ka[0], ka[1]), pk2(ka[2], ka[3])});
                const f32x4 qa = (acc[t][0] + accc[0] * nm[t + 1]) * rsd[t + 1];
                qf[i] = __builtin_bit_cast(s16x4, u32x2{pk2(qa[0], qa[1]), pk2(qa[2], qa[3])});
            }
            f32x4 sp[4], sc[4];
#pragma unroll
            for (int i = 0; i < 4; ++i) {
                sp[i] = __builtin_amdgcn_mfma_f32_16x16x16bf16_1k(kf[i], qf[i], z, 0, 0, 0);
                sc[i] = __builtin_amdgcn_mfma_f32_16x16x16bf16_1k(kf[i + 1], qf[i], z, 0, 0, 0);
            }
#pragma unroll
            for (int i = 0; i < 4; ++i) {
                const f32x4 va = (acc[4 * hb + i][2] + vm[i] * csv) * vr[i];
                vf[i + 1] = u32x2{pk2(va[0], va[1]), pk2(va[2], va[3])};
            }
            kprev = kf[4]; vprev = vf[4];
            unsigned lw[4];
#pragma unroll
            for (int i = 0; i < 4; ++i) {
                asm volatile("" : "+v"(sp[i]), "+v"(sc[i]));
                const float a0 = par ? sc[i][1] : sc[i][0], a1 = par ? sp[i][1] : sp[i][0];
                const float b0 = par ? sc[i][3] : sc[i][2], b1 = par ? sp[i][3] : sp[i][2];
                float la = ca ? a0 : a1, lb = cb ? b0 : b1;
                if (i == 0 && hb == 0) { if (p == 0) { la = ca ? la : -3.0e38f; lb = cb ? lb : -3.0e38f; } }
                lw[i] = pk2(la, lb);
            }
            tr4(lw);
            float e8[8];
#pragma unroll
            for (int i = 0; i < 4; ++i) { e8[2 * i] = bflo(lw[i]); e8[2 * i + 1] = bfhi(lw[i]); }
            const float mx = fmaxf(fmaxf(fmaxf(e8[0], e8[1]), fmaxf(e8[2], e8[3])), fmaxf(fmaxf(e8[4], e8[5]), fmaxf(e8[6], e8[7])));
            float sum = 0;
#pragma unroll
            for (int i = 0; i < 8; ++i) { e8[i] = __builtin_amdgcn_exp2f((e8[i] - mx) * (0.25f * LOG2E)); sum += e8[i]; }
            const float inv = __builtin_amdgcn_rcpf(sum);
#pragma unroll
            for (int i = 0; i < 4; ++i) lw[i] = pk2(e8[2 * i] * inv, e8[2 * i + 1] * inv);
            tr4(lw);
#pragma unroll
            for (int i = 0; i < 4; ++i) {
                const unsigned pw = lw[i];
                const unsigned wa = par ? (pw << 16) : (pw & 0xffffu), wb = par ? (pw & 0xffff0000u) : (pw >> 16);
                const u32x4 pp = {ca ? 0u : wa, cb ? 0u : wb, ca ? wa : 0u, cb ? wb : 0u};
                const u32x4 vv = {vf[i][0], vf[i][1], vf[i + 1][0], vf[i + 1][1]};
                sp[i] = mfma32(__builtin_bit_cast(bf16x8, vv), __builtin_bit_cast(bf16x8, pp), z);
            }
#pragma unroll
            for (int i = 0; i < 4; ++i) LDS(u32x2, ow + 16 * (4 * hb + i) * OST + j * 32) = u32x2{pk2(sp[i][0], sp[i][1]), pk2(sp[i][2], sp[i][3])};
        }
    } else {
        const int jj = j0 - 8, odd = jj & 1;
        const int lo_off = (odd ? 64 : 0) + j * 64;
        const int mx_off = (odd ? 0 : 256) + j * 16;
#pragma unroll
        for (int i = 0; i < 8; ++i) {
            const float r2 = rsd[i + 1] * rsd[i + 1];
#pragma unroll
            for (int c = 0; c < 2; ++c)
                LDS(u32x2, ow + 16 * i * OST + lo_off + c * 32) = hmul(acc[i][c] + accc[c] * nm[i + 1], acc[i][c + 2] + accc[c + 2] * nm[i + 1], r2);
        }
#pragma unroll
        for (int i = 0; i < 8; i += 2) {
            const f32x4 xa = (acc[i][4] + accc[4] * nm[i + 1]) * rsd[i + 1], ya = (acc[i + 1][4] + accc[4] * nm[i + 2]) * rsd[i + 2];
            f32x4 ga, ua;
#pragma unroll
            for (int e = 0; e < 4; ++e) {
                unsigned a = __float_as_uint(xa[e]), b = __float_as_uint(ya[e]);
                swap32(a, b);
                ga[e] = __uint_as_float(a); ua[e] = __uint_as_float(b);
            }
            LDS(u32x2, L_A + (16 * (i + (g >> 1)) + tok) * OST + mx_off + (g & 1) * 8) = hmul(ga, ua, 1.f);
        }
    }
}

__device__ __forceinline__ void store_a2(u16* a2, bool TA, int p, int j0) {
    int tid = threadIdx.x;
    asm volatile("" : "+v"(tid));
    bar_lgkm();
    const __amdgpu_buffer_rsrc_t a2r = __builtin_amdgcn_make_buffer_rsrc(a2, 0, S_ * A2LD * 2, 0x00020000);
    if (TA) {
        const int ch = tid & 15;
        const unsigned rd = L_A + (tid >> 4) * 272 + ch * 16;
        const unsigned go = ((p * 128 + (tid >> 4)) * A2LD + 64 * j0 + (ch & 7) * 8 + (ch >> 3) * 512) * 2;
        u32x4 v[4];
#pragma unroll
        for (int it = 0; it < 4; ++it) v[it] = LDS(const u32x4, rd + it * 32 * 272);
#pragma unroll
        for (int it = 0; it < 4; ++it) __builtin_amdgcn_raw_buffer_store_b128(v[it], a2r, go, it * 32 * A2LD * 2, 16);
    } else {
        const int row = tid >> 2, c0 = tid & 3;
        const unsigned rd = L_A + row * 336 + c0 * 16;
        const unsigned go = ((p * 128 + row) * A2LD + 1024 + 160 * (j0 - 8) + c0 * 8) * 2;
        u32x4 v[5];
#pragma unroll
        for (int it = 0; it < 5; ++it) v[it] = LDS(const u32x4, rd + it * 64);
#pragma unroll
        for (int it = 0; it < 5; ++it) __builtin_amdgcn_raw_buffer_store_b128(v[it], a2r, go + it * 64, 0, 16);
    }
}

__global__ __launch_bounds__(512, 2) void kfused(const u16* x, const u16* wqkv, const u16* wo, const u16* wg, const u16* wu, const u16* wd,
                                                 u16* a2, u16* out, unsigned* flags) {
    const int w = __builtin_amdgcn_readfirstlane(threadIdx.x >> 6);
    const int b = blockIdx.x, xcd = b & 7, loc = b >> 3;
    const int ci = loc >> 3;
    const int p = (xcd & 1) * 8 + (loc & 7), qd = xcd >> 1;
    const int j0 = (ci < 2) ? 2 * qd + ci : 6 + 2 * qd + ci;
    const int rt = (xcd & 1) * 16 + (xcd >> 1) * 4 + (loc & 3), ct = loc >> 2;
    const int R0 = rt * 64, C0 = ct * 64;
    const bool TA = ci < 2;
    if (w >= 4) {
        const int l = threadIdx.x & 63, kq = l >> 3;
        unsigned* const myP = flags + p * 32 + j0;
        const unsigned* const myE = flags + 512 + (p * 16 + j0) * 32 + (l & 15);
        const unsigned* const pollP = flags + (rt >> 1) * 32 + (l & 15);
        unsigned* const echoE = flags + 512 + ((rt >> 1) * 16 + (l & 15)) * 32 + (rt & 1) * 8 + ct;
        unsigned myv = 0, last = 1;
        if (w == 4) touch_v_sc1(myE, L_FE);
        if (TA) p1_helper<true>(x, wqkv, wo, wg, wu, wd, myP, myE, pollP, echoE, p, j0, C0, myv, last);
        else p1_helper<false>(x, wqkv, wo, wg, wu, wd, myP, myE, pollP, echoE, p, j0, C0, myv, last);
        store_a2(a2, TA, p, j0);
        const int h = w - 4;
        unsigned vA[2], vB[4];
        {
            const int row = 8 * h + kq;
            vA[0] = (unsigned)(row * (A2LD * 2) + (((l & 7) ^ ((row >> 1) & 7)) * 16));
            vA[1] = vA[0] + 32 * A2LD * 2;
            const int lch = (l & 7) ^ ((((row >> 1) & 1) | ((h & 1) << 1)) << 1);
            vB[0] = (unsigned)(row * 1024 + lch * 16);
            vB[1] = vB[0] + 32768; vB[2] = vB[0] + 65536; vB[3] = vB[0] + 98304;
        }
        const char* sa = (const char*)(a2 + (size_t)R0 * A2LD);
        const char* sb = (const char*)(wd + C0) + 131072;
        const unsigned dA2 = L_A + h * 1024, dW2 = L_B + h * 1024;
        wait_bar<0>();
        if (h == 0) {
            unsigned ev = LDS(const volatile unsigned, L_FE + l * 4), pv;
            unsigned long long pend = 1ull;
#pragma unroll 1
            for (unsigned spin = 0; spin < SPIN_CAP; ++spin) {
                const unsigned long long go = (__ballot(ev != myv) == 0ull) ? pend : 0ull;
                st_sc1(myP, myv + 1, go);
                pend &= ~go;
                ld2_sc1(pv, pollP, ev, myE);
                st_sc1(echoE, pv, __ballot(!(pv & 1) && pv != last) & 0xffffull);
                last = (pv & 1) ? last : pv;
                const unsigned long long notdone = __ballot(!((pv == last + 1) && (pv & 1)));
                if ((pend | notdone) == 0ull) break;
                __builtin_amdgcn_s_sleep(1);
            }
            wait_vm<0>();
        }
        bar();
        auto issA2 = [&](unsigned d) {
            setm0<0>(d); nop1(); dma_s_sc1<0>(sa, vA[0]);
            setm0<4096>(d); nop1(); dma_s_sc1<0>(sa, vA[1]);
            setm0<8192 - 128>(d); nop1(); dma_s_sc1<128>(sa, vA[0]);
            setm0<12288 - 128>(d); nop1(); dma_s_sc1<128>(sa, vA[1]);
            sa += 256;
        };
        issA2(dA2); issA2(dA2 + ABLK);
        wait_vm<4>(); bar();
        issA2(dA2 + 2 * ABLK);
        unsigned as = 0, bs = BBLK;
        wait_vm<4>(); bar();
#pragma unroll 1
        for (int st = 0; st < 16; ++st) {
            if (st < 13) {
                setm0<0>(dW2 + bs); nop1(); dma_s<0>(sb, vB[0]);
                setm0<4096>(dW2 + bs); nop1(); dma_s<0>(sb, vB[1]);
                setm0<8192>(dW2 + bs); nop1(); dma_s<0>(sb, vB[2]);
                setm0<12288>(dW2 + bs); nop1(); dma_s<0>(sb, vB[3]);
                sb += 131072;
            }
            if (st < 15) issA2(dA2 + as);
            bs = (bs == (NB - 1) * BBLK) ? 0u : bs + BBLK;
            as = (as == (NA - 1) * ABLK) ? 0u : as + ABLK;
            if (st < 13) wait_vm<8>(); else if (st < 15) wait_vm<4>(); else wait_vm<0>();
            bar();
        }
        return;
    }
    if (TA) p1_mfma<true>(p, j0); else p1_mfma<false>(p, j0);
    store_a2(a2, TA, p, j0);
    wait_bar<0>();
    {
        int l = threadIdx.x & 63;
        asm volatile("" : "+v"(l));
        const int g = l >> 4, tok = l & 15;
        const int wm = w >> 1, wn = w & 1;
        const unsigned a2A = L_A + (32 * wm + tok) * 128 + ((g ^ (tok >> 1)) * 16);
        const unsigned a2B = L_B + (8 * g + (tok >> 2)) * 128 + (((2 * wn) ^ (((tok >> 3) & 1) | ((g & 1) << 1))) * 32) + (l & 3) * 8;
        const size_t orow = (size_t)(R0 + 32 * wm + tok) * D_ + C0 + 32 * wn + 4 * g;
        u32x2 xv[2][2];
#pragma unroll
        for (int i = 0; i < 2; ++i)
#pragma unroll
            for (int c = 0; c < 2; ++c) xv[i][c] = *(const u32x2*)(x + orow + i * 16 * D_ + 16 * c);
        const char* tA;
        { const int id = ct * 288 + w * 64 + l; tA = (const char*)(a2 + (size_t)(R0 + (id & 63)) * A2LD) + (id >> 6) * 128; }
        const char* tA2;
        { const int id = ct * 288 + 256 + (l & 31); tA2 = (const char*)(a2 + (size_t)(R0 + (id & 63)) * A2LD) + (id >> 6) * 128; }
        f32x4 c2[2][2][2];
#pragma unroll
        for (int a = 0; a < 2; ++a)
#pragma unroll
            for (int i = 0; i < 2; ++i)
#pragma unroll
                for (int c = 0; c < 2; ++c) c2[a][i][c] = f32x4{0, 0, 0, 0};
        bf16x8 ga[2][4][2], gb[2][4][2];
        unsigned b2o = BBLK, a2o = 0;
        unsigned pa0 = a2A, pa1 = pa0 ^ 64, pb0 = a2B + b2o, pb1 = pb0 ^ 32;
        auto rd = [&](auto SET, auto I) {
            constexpr int set = SET, i = I, ks = i >> 1;
            ga[set][ks][i & 1] = lds_a(((ks & 1) ? pa1 : pa0) + (ks >> 1) * 8192 + (i & 1) * 2048);
            gb[set][ks][i & 1] = lds_b(((i & 1) ? pb1 : pb0) + ks * 4096);
        };
        auto adv = [&]() {
            b2o = (b2o == (NB - 1) * BBLK) ? 0u : b2o + BBLK;
            a2o = (a2o == (NA - 1) * ABLK) ? 0u : a2o + ABLK;
            pa0 = a2A + a2o; pa1 = pa0 ^ 64; pb0 = a2B + b2o; pb1 = pb0 ^ 32;
            asm volatile("" : "+v"(pa0), "+v"(pa1), "+v"(pb0), "+v"(pb1));
        };
        auto stage = [&](auto SET, auto ACC, auto LAST) {
            constexpr int set = SET, ac = ACC; constexpr bool lastst = LAST;
            if constexpr (!lastst) bar_lgkm();
            SB();
            sfor<16>([&](auto I) {
                constexpr int idx = I, ks = idx >> 2, i = (idx >> 1) & 1, c = idx & 1;
                c2[ac][i][c] = mfma32(gb[set][ks][c], ga[set][ks][i], c2[ac][i][c]);
                if constexpr (!lastst && idx < 8) rd(IC(set ^ 1), I);
                if constexpr (!lastst && idx == 10) adv();
                SB();
            });
        };
        typedef std::integral_constant<bool, true> T_; typedef std::integral_constant<bool, false> F_t;
        asm volatile("s_waitcnt vmcnt(0)" : "+v"(xv[0][0]), "+v"(xv[0][1]), "+v"(xv[1][0]), "+v"(xv[1][1]) :: "memory");
        bar();
        touch_v_sc1(tA, L_DUMP);
        if (w == 0) touch_v_sc1(tA2, L_DUMP);
        bar();
        sfor<8>([&](auto I) { rd(IC(0), I); });
        adv();
#pragma unroll 1
        for (int it = 0; it < 2; ++it) { stage(IC(0), IC(0), F_t{}); stage(IC(1), IC(0), F_t{}); }
#pragma unroll 1
        for (int it = 2; it < 8; ++it) {
            stage(IC(0), IC(1), F_t{}); stage(IC(1), IC(1), F_t{});
        }
        stage(IC(0), IC(1), F_t{});
        stage(IC(1), IC(1), T_{});
#pragma unroll
        for (int i = 0; i < 2; ++i)
#pragma unroll
            for (int c = 0; c < 2; ++c) {
                float r[4];
#pragma unroll
                for (int e = 0; e < 4; ++e) {
                    const float d = rbf(c2[1][i][c][e]);
                    const float sg = rbf(d * __builtin_amdgcn_rcpf(1.f + __builtin_amdgcn_exp2f(-d * LOG2E)));
                    const float t = rbf(rbf(c2[0][i][c][e]) * sg);
                    const float xx = (e & 1) ? bfhi(xv[i][c][e >> 1]) : bflo(xv[i][c][e >> 1]);
                    r[e] = xx + t;
                }
                *(u32x2*)(out + orow + i * 16 * D_ + 16 * c) = u32x2{pk2(r[0], r[1]), pk2(r[2], r[3])};
            }
    }
}

extern "C" void kernel_launch(void* const* d_in, const int* in_sizes, int n_in,
                              void* d_out, int out_size, void* d_ws, size_t ws_size,
                              hipStream_t stream) {
    (void)hipFuncSetAttribute((const void*)kfused, hipFuncAttributeMaxDynamicSharedMemorySize, K_LDS);
    kfused<<<256, 512, K_LDS, stream>>>((const u16*)d_in[0], (const u16*)d_in[1], (const u16*)d_in[2], (const u16*)d_in[4], (const u16*)d_in[5],
                                        (const u16*)d_in[6], (u16*)d_ws, (u16*)d_out, (unsigned*)((char*)d_ws + (16 << 20)));
}
```

```cpp
#include <hip/hip_runtime.h>
#include <stdint.h>
#include <utility>

typedef unsigned short u16;
typedef __attribute__((ext_vector_type(8))) __bf16 bf16x8;
typedef __attribute__((ext_vector_type(2))) __bf16 bf16x2;
typedef __attribute__((ext_vector_type(4))) short s16x4;
typedef __attribute__((ext_vector_type(8))) short s16x8;
typedef __attribute__((ext_vector_type(4))) float f32x4;
typedef __attribute__((ext_vector_type(2))) float f32x2;
typedef __attribute__((ext_vector_type(4))) unsigned u32x4;
typedef __attribute__((ext_vector_type(2))) unsigned u32x2;
#define LAS __attribute__((address_space(3)))
#define SB() __builtin_amdgcn_sched_barrier(0)
#define IC(n) std::integral_constant<int, (n)>{}
#define LDS(T, a) (*(LAS T*)(uintptr_t)(a))

constexpr int S_ = 2048, D_ = 512, F_ = 1792;
constexpr int A2LD = 2304;
constexpr float LOG2E = 1.4426950408889634f;

template <int... I, class Fn> __device__ __forceinline__ void sfor_impl(std::integer_sequence<int, I...>, Fn&& f) { (f(std::integral_constant<int, I>{}), ...); }
template <int N, class Fn> __device__ __forceinline__ void sfor(Fn&& f) { sfor_impl(std::make_integer_sequence<int, N>{}, f); }

__device__ __forceinline__ float rbf(float f) { return (float)(__bf16)f; }
__device__ __forceinline__ unsigned pk2(float lo, float hi) {
    bf16x2 v; v[0] = (__bf16)lo; v[1] = (__bf16)hi; return __builtin_bit_cast(unsigned, v);
}
__device__ __forceinline__ float bflo(unsigned u) { return __uint_as_float(u << 16); }
__device__ __forceinline__ float bfhi(unsigned u) { return __uint_as_float(u & 0xffff0000u); }

template <int OFF> __device__ __forceinline__ void setm0(unsigned base) {
    asm volatile("s_add_u32 m0, %0, %1" :: "s"(base), "n"(OFF) : "scc");
}
__device__ __forceinline__ void dma_v(const void* g) { asm volatile("global_load_lds_dwordx4 %0, off" :: "v"(g) : "memory"); }
__device__ __forceinline__ void dma_vm(const void* g, unsigned long long mask) {
    asm volatile("s_mov_b64 exec, %1\n\tglobal_load_lds_dwordx4 %0, off\n\ts_mov_b64 exec, -1" :: "v"(g), "s"(mask) : "memory");
}
template <int IO> __device__ __forceinline__ void dma_s(const void* sb, unsigned vo) {
    asm volatile("global_load_lds_dwordx4 %0, %1 offset:%2" :: "v"(vo), "s"(sb), "n"(IO) : "memory");
}
template <int IO> __device__ __forceinline__ void dma_s_sc1(const void* sb, unsigned vo) {
    asm volatile("global_load_lds_dwordx4 %0, %1 offset:%2 sc1" :: "v"(vo), "s"(sb), "n"(IO) : "memory");
}
template <int LIM> __device__ __forceinline__ void dma_s_c(int cnt, const void* sb, unsigned vo) {
    asm volatile("s_cmp_ge_i32 %2, %3\n\ts_cbranch_scc1 1f\n\tglobal_load_lds_dwordx4 %0, %1\n1:" :: "v"(vo), "s"(sb), "s"(cnt), "n"(LIM) : "memory", "scc");
}
template <int IO> __device__ __forceinline__ void dma_s32_sc1(const void* sb, unsigned vo) {
    asm volatile("s_mov_b64 exec, %3\n\tglobal_load_lds_dwordx4 %0, %1 offset:%2 sc1\n\ts_mov_b64 exec, -1" :: "v"(vo), "s"(sb), "n"(IO), "s"(0xffffffffull) : "memory");
}
__device__ __forceinline__ void touch_s(const void* sb, unsigned vo) {
    asm volatile("global_load_lds_dword %0, %1" :: "v"(vo), "s"(sb) : "memory");
}
__device__ __forceinline__ unsigned ld_sc1(const unsigned* p) {
    unsigned a; asm volatile("global_load_dword %0, %1, off sc1\n\ts_waitcnt vmcnt(0)" : "=&v"(a) : "v"(p) : "memory"); return a;
}
template <int LIM> __device__ __forceinline__ void dma_s16_c(int cnt, const void* sb, unsigned vo) {
    asm volatile("s_cmp_ge_i32 %2, %3\n\ts_cbranch_scc1 1f\n\ts_mov_b64 exec, 0xffff\n\tglobal_load_lds_dwordx4 %0, %1\n\ts_mov_b64 exec, -1\n1:"
                 :: "v"(vo), "s"(sb), "s"(cnt), "n"(LIM) : "memory", "scc");
}
__device__ __forceinline__ void nop1() { asm volatile("s_nop 0"); }
__device__ __forceinline__ void touch_v(const void* gaddr, unsigned ldsaddr) {
    asm volatile("s_mov_b32 m0, %1\n\ts_nop 0\n\tglobal_load_lds_dword %0, off" :: "v"(gaddr), "s"(ldsaddr) : "memory");
}
__device__ __forceinline__ void touch_v_sc1(const void* gaddr, unsigned ldsaddr) {
    asm volatile("s_mov_b32 m0, %1\n\ts_nop 0\n\tglobal_load_lds_dword %0, off sc1" :: "v"(gaddr), "s"(ldsaddr) : "memory");
}
template <int N> __device__ __forceinline__ void wait_vm() { asm volatile("s_waitcnt vmcnt(%0)" :: "n"(N) : "memory"); }
template <int N> __device__ __forceinline__ void wait_bar() {
    __builtin_amdgcn_s_waitcnt(0xC07F);
    asm volatile("s_waitcnt vmcnt(%0)\n\ts_barrier" :: "n"(N) : "memory");
}
__device__ __forceinline__ void bar_lgkm() { __builtin_amdgcn_s_waitcnt(0xC07F); asm volatile("s_barrier" ::: "memory"); }
__device__ __forceinline__ void bar() { asm volatile("s_barrier" ::: "memory"); }
__device__ __forceinline__ void ld2_sc1(unsigned& a, const unsigned* pa, unsigned& b, const unsigned* pb) {
    asm volatile("global_load_dword %0, %2, off sc1\n\tglobal_load_dword %1, %3, off sc1\n\ts_waitcnt vmcnt(0)"
                 : "=&v"(a), "=&v"(b) : "v"(pa), "v"(pb) : "memory");
}
__device__ __forceinline__ void st_sc1(unsigned* p, unsigned v, unsigned long long mask) {
    asm volatile("s_mov_b64 exec, %2\n\ts_cbranch_execz 1f\n\tglobal_store_dword %0, %1, off sc1\n1:\n\ts_mov_b64 exec, -1"
                 :: "v"(p), "v"(v), "s"(mask) : "memory");
}

__device__ __forceinline__ bf16x8 lds_a(unsigned addr) { return LDS(const bf16x8, addr); }
__device__ __forceinline__ bf16x8 lds_b(unsigned addr) {
    s16x4 lo = __builtin_amdgcn_ds_read_tr16_b64_v4i16((LAS s16x4*)(uintptr_t)addr);
    s16x4 hi = __builtin_amdgcn_ds_read_tr16_b64_v4i16((LAS s16x4*)(uintptr_t)(addr + 512));
    s16x8 r = __builtin_shufflevector(lo, hi, 0, 1, 2, 3, 4, 5, 6, 7);
    return __builtin_bit_cast(bf16x8, r);
}
__device__ __forceinline__ f32x4 mfma32(bf16x8 a, bf16x8 b, f32x4 c) { return __builtin_amdgcn_mfma_f32_16x16x32_bf16(a, b, c, 0, 0, 0); }
__device__ __forceinline__ void swap16(unsigned& a, unsigned& b) { auto r = __builtin_amdgcn_permlane16_swap(a, b, false, false); a = r[0]; b = r[1]; }
__device__ __forceinline__ void swap32(unsigned& a, unsigned& b) { auto r = __builtin_amdgcn_permlane32_swap(a, b, false, false); a = r[0]; b = r[1]; }
__device__ __forceinline__ void tr4(unsigned (&v)[4]) { swap16(v[0], v[1]); swap16(v[2], v[3]); swap32(v[0], v[2]); swap32(v[1], v[3]); }
__device__ __forceinline__ float xadd16(float v) { unsigned a = __float_as_uint(v), b = a; swap16(a, b); return __uint_as_float(a) + __uint_as_float(b); }
template <int CTRL> __device__ __forceinline__ float dadd(float v) {
    return v + __int_as_float(__builtin_amdgcn_update_dpp(0, __float_as_int(v), CTRL, 0xf, 0xf, true));
}
__device__ __forceinline__ float xadd32(float v) { unsigned a = __float_as_uint(v), b = a; swap32(a, b); return __uint_as_float(a) + __uint_as_float(b); }
__device__ __forceinline__ float red8(float v) { return dadd<0x141>(dadd<0x4E>(dadd<0xB1>(v))); }
__device__ __forceinline__ float red64(float v) { return xadd32(xadd16(dadd<0x140>(red8(v)))); }

constexpr int NB = 5, NA = 3;
constexpr int BBLK = 20480, ABLK = 18432;
constexpr int L_B = 0, L_A = NB * BBLK, L_ST = L_A + NA * ABLK;
constexpr int L_CNT = L_ST;
constexpr int L_DUMP = L_ST + 64;
constexpr int L_FE = L_DUMP + 256, L_FP = L_FE + 256;
constexpr int K_LDS = L_FP + 256;
constexpr unsigned SPIN_CAP = 1u << 16;
#define CODE_LINES 200
#define TOUCH_AT 2

__device__ __forceinline__ void ld_row(unsigned dst, const u16* x, int p, int ir) {
    int gr = p * 128 - 16 + ir; gr = gr < 0 ? 0 : gr;
    setm0<0>(dst); nop1(); dma_s<0>(x + (size_t)gr * 512, (threadIdx.x & 63) * 16);
}
__device__ __forceinline__ void ln_row(u32x4 v, u16* xn, int p, int ir, bool real) {
    float f[8], s = 0;
#pragma unroll
    for (int e = 0; e < 4; ++e) { const unsigned u = v[e]; f[2 * e] = bflo(u); f[2 * e + 1] = bfhi(u); s += f[2 * e] + f[2 * e + 1]; }
    const float sh = __int_as_float(__builtin_amdgcn_readfirstlane(__float_as_int(f[0])));
    float q = 0;
#pragma unroll
    for (int e = 0; e < 8; ++e) { const float d = f[e] - sh; q = fmaf(d, d, q); }
    const float mean = red64(s) * (1.f / 512.f);
    const float dm = mean - sh;
    const float var = fmaxf(red64(q) * (1.f / 512.f) - dm * dm, 0.f);
    const float sd = rbf(__builtin_amdgcn_sqrtf(rbf(rbf(var) + 1e-5f)));
    const float mb = rbf(mean), rc = __builtin_amdgcn_rcpf(sd);
    float o[8];
#pragma unroll
    for (int e = 0; e < 8; ++e) {
        const float a = rbf(f[e] - mb);
        const float q0 = a * rc;
        o[e] = fmaf(fmaf(-q0, sd, a), rc, q0);
    }
    const u32x4 ov = {pk2(o[0], o[1]), pk2(o[2], o[3]), pk2(o[4], o[5]), pk2(o[6], o[7])};
    const __amdgpu_buffer_rsrc_t xr = __builtin_amdgcn_make_buffer_rsrc(xn, 0, 16 * 144 * 1024, 0x00020000);
    if (real) __builtin_amdgcn_raw_buffer_store_b128(ov, xr, (unsigned)((p * 144 + ir) * 1024 + (threadIdx.x & 63) * 16), 0, 16);
    else asm volatile("" :: "v"(ov));
}
__device__ __forceinline__ void ln_done() {
    wait_vm<0>();
    if ((threadIdx.x & 63) == 0) __hip_atomic_fetch_add((LAS unsigned*)(uintptr_t)L_CNT, 1u, __ATOMIC_RELAXED, __HIP_MEMORY_SCOPE_WORKGROUP);
}

__device__ __forceinline__ void ln_exchange(u16* xn, unsigned* inbox, int w, int p, int j0) {
    const int l = threadIdx.x & 63;
    const unsigned* in = inbox + (p * 16 + j0) * 32 + (l & 31);
    unsigned* outp = inbox + (p * 16 + (l & 15)) * 32 + ((l & 16) ? j0 : 16 + j0);
    unsigned want = 0;
    const int npass = (w == 0) ? 3 : 2;
#pragma unroll 1
    for (int pass = (w < 4) ? 0 : 1; pass < npass; ++pass) {
        if (pass == 1) {
            if (w < 4) wait_vm<0>(); else wait_vm<5>();
            if (w == 3) {
                want = LDS(const volatile unsigned, L_FP + l * 4) + 1u;
                st_sc1(outp, want, 0xffff0000ull);
            }
        }
        const u32x4 v = LDS(const volatile u32x4, L_A + (pass == 2 ? 8 : w) * 1024 + l * 16);
        ln_row(v, xn, p, pass == 2 ? j0 : 16 + 8 * j0 + w, pass != 0);
    }
    ln_done();
    if (w != 3) return;
    unsigned u = ld_sc1(in);
    unsigned spin = 0;
#pragma unroll 1
    while (__builtin_amdgcn_readfirstlane(LDS(const volatile unsigned, L_CNT)) != 8u && ++spin < (SPIN_CAP << 4)) {}
    st_sc1(outp, u, 0xffffull);
    bool go = false;
#pragma unroll 1
    for (spin = 0; spin < SPIN_CAP; ++spin) {
        const unsigned echoed = u;
        u = ld_sc1(in);
        st_sc1(outp, u, __ballot(u != echoed) & 0xffffull);
        if (go) break;
        if ((__ballot(u != want) & 0xffff0000ull) == 0ull) { go = true; bar(); }
    }
    if (!go) bar();
}
__device__ __forceinline__ void store_h(u16* a2, bool TA, int p, int j0) {
    int tid = threadIdx.x;
    asm volatile("" : "+v"(tid));
    const __amdgpu_buffer_rsrc_t a2r = __builtin_amdgcn_make_buffer_rsrc(a2, 0, S_ * A2LD * 2, 0x00020000);
    if (TA) {
        const int t4 = tid & 255;
        const unsigned rd = L_A + (t4 >> 3) * 144 + (t4 & 7) * 16;
        const unsigned go = ((p * 128 + (t4 >> 3)) * A2LD + 512 + 64 * j0 + (t4 & 7) * 8) * 2;
        bar_lgkm();
        u32x4 v[4];
#pragma unroll
        for (int it = 0; it < 4; ++it) v[it] = LDS(const u32x4, rd + it * 32 * 144);
#pragma unroll
        for (int it = 0; it < 4; ++it) __builtin_amdgcn_raw_buffer_store_b128(v[it], a2r, go, it * 32 * A2LD * 2, 16);
    } else {
        unsigned rd[5], go[5];
#pragma unroll
        for (int it = 0; it < 5; ++it) {
            const int id = it * 512 + tid, r_ = id / 20, c_ = id - r_ * 20;
            rd[it] = L_A + r_ * 336 + c_ * 16;
            go[it] = ((p * 128 + r_) * A2LD + 1024 + 160 * (j0 - 8) + c_ * 8) * 2;
        }
        bar_lgkm();
        u32x4 v[5];
#pragma unroll
        for (int it = 0; it < 5; ++it) v[it] = LDS(const u32x4, rd[it]);
#pragma unroll
        for (int it = 0; it < 5; ++it) __builtin_amdgcn_raw_buffer_store_b128(v[it], a2r, go[it], 0, 16);
    }
    if ((tid & 63) == 0) __hip_atomic_fetch_add((LAS unsigned*)(uintptr_t)(L_CNT + 12), 1u, __ATOMIC_RELAXED, __HIP_MEMORY_SCOPE_WORKGROUP);
    wait_vm<0>();
    if ((tid & 63) == 0) __hip_atomic_fetch_add((LAS unsigned*)(uintptr_t)(L_CNT + 4), 1u, __ATOMIC_RELAXED, __HIP_MEMORY_SCOPE_WORKGROUP);
}
__device__ __forceinline__ void lds_wait(int addr, unsigned val) {
    unsigned spin = 0;
#pragma unroll 1
    while (__builtin_amdgcn_readfirstlane(LDS(const volatile unsigned, addr)) != val && ++spin < (SPIN_CAP << 6)) {}
}

template <bool TA>
__device__ __forceinline__ void p1_mfma(u16* a2, int p, int j0) {
    int l = threadIdx.x & 63;
    asm volatile("" : "+v"(l));
    const int j = __builtin_amdgcn_readfirstlane(threadIdx.x >> 6);
    const int g = l >> 4, tok = l & 15;
    const unsigned adA0 = L_A + tok * 128 + ((g ^ (tok >> 1)) * 16);
    const int f2 = ((tok >> 3) & 1) | ((g & 1) << 1);
    const unsigned comB = L_B + (8 * g + (tok >> 2)) * 128;
    unsigned adB0, adB1 = 0;
    {
        const int pp = l & 3;
        if (TA) adB0 = comB + ((j ^ f2) * 32) + pp * 8;
        else {
            adB0 = comB + (j >> 1) * 4096 + (((2 * (j & 1)) ^ f2) * 32) + pp * 8;
            adB1 = comB + 2 * 4096 + ((((pp >> 1) * 2 + (j >> 1)) ^ f2) * 32) + (j & 1) * 16 + (pp & 1) * 8;
        }
    }
    f32x4 acc[8][5], acch[2];
#pragma unroll
    for (int i = 0; i < 8; ++i)
#pragma unroll
        for (int c = 0; c < 5; ++c) { acc[i][c] = f32x4{0, 0, 0, 0}; asm volatile("" : "+v"(acc[i][c])); }
    acch[0] = acch[1] = f32x4{0, 0, 0, 0};
    asm volatile("" : "+v"(acch[0]), "+v"(acch[1]));
    bf16x8 fa[6], fb[5];
    unsigned ra = adA0, rb0 = adB0, rb1 = adB1, rb2 = adB0 ^ 32;
    unsigned bo = 0, ao = 0;
    auto rdB = [&](auto R) {
        constexpr int r = R;
        if constexpr (TA) fb[r] = lds_b(rb0 + r * 4096);
        else fb[r] = (r == 0) ? lds_b(rb0) : (r == 1) ? lds_b(rb2) : (r == 2) ? lds_b(rb0 + 3 * 4096) : (r == 3) ? lds_b(rb2 + 3 * 4096) : lds_b(rb1);
    };
    auto mm = [&](auto I, auto C, auto SL) {
        constexpr int i = I, c = C, sl = SL;
        if constexpr (TA && c == 2) acc[i][c] = mfma32(fa[sl], fb[c], acc[i][c]);
        else acc[i][c] = mfma32(fb[c], fa[sl], acc[i][c]);
    };
    auto step = [&](auto PAR) {
        constexpr int par = PAR, X = par ? 3 : 0, Y = par ? 0 : 3;
        sfor<15>([&](auto I) {
            constexpr int idx = I, c = idx / 3, i = idx % 3;
            mm(IC(i), IC(c), IC(X + i));
            if constexpr (idx == 0) fa[Y] = lds_a(ra + 4 * 2048);
            if constexpr (idx == 1) fa[Y + 1] = lds_a(ra + 5 * 2048);
            if constexpr (idx == 2 && TA) fa[Y + 2] = lds_a(ra);
            if constexpr (idx == 4) { bo = (bo == (NB - 1) * BBLK) ? 0u : bo + BBLK; if constexpr (par == 1) ao = (ao == (NA - 1) * ABLK) ? 0u : ao + ABLK; asm volatile("" : "+s"(bo), "+s"(ao)); }
            if constexpr (idx == 6) { rb0 = adB0 + bo; asm volatile("" : "+v"(rb0)); }
            if constexpr (idx == 8 && !TA) { rb1 = adB1 + bo; asm volatile("" : "+v"(rb1)); }
            if constexpr (idx == 10 && !TA) { rb2 = rb0 ^ 32; asm volatile("" : "+v"(rb2)); }
            SB();
        });
        sfor<10>([&](auto I) {
            constexpr int idx = I, c = idx / 2, i = idx % 2;
            mm(IC(3 + i), IC(c), IC(Y + i));
            if constexpr (idx < 3) fa[X + idx] = lds_a(ra + (6 + idx) * 2048);
            if constexpr (idx == 4) { ra = (par == 0) ? (ra ^ 64) : adA0 + ao; asm volatile("" : "+v"(ra)); }
            SB();
        });
        if constexpr (TA) {
            acch[0] = mfma32(fb[1], fa[Y + 2], acch[0]);
            acch[1] = mfma32(fa[Y + 2], fb[2], acch[1]);
        }
        SB();
        bar_lgkm();
        SB();
        sfor<5>([&](auto C) {
            constexpr int c = C;
            mm(IC(5), C, IC(X));
            if constexpr (c < 3) fa[Y + c] = lds_a(ra + (1 + c) * 2048);
            SB();
            mm(IC(6), C, IC(X + 1));
            mm(IC(7), C, IC(X + 2));
            SB();
            rdB(C);
            SB();
        });
    };
    __builtin_amdgcn_s_setprio(1);
    SB();
    bar_lgkm();
    SB();
    sfor<5>([&](auto C) {
        constexpr int c = C;
        if constexpr (c < 3) fa[c] = lds_a(ra + (1 + c) * 2048);
        SB();
        rdB(C);
        SB();
    });
#pragma unroll 1
    for (int s = 0; s < 16; s += 2) {
        step(IC(0));
        step(IC(1));
    }
    __builtin_amdgcn_s_setprio(0);
    constexpr int OST = TA ? 144 : 336;
    const unsigned ow = L_A + tok * OST + g * 8;
    auto cvt4 = [&](f32x4 a) { return u32x2{pk2(a[0], a[1]), pk2(a[2], a[3])}; };
    auto hmul = [&](f32x4 ga, f32x4 ua) {
        f32x4 t;
#pragma unroll
        for (int e = 0; e < 4; ++e) t[e] = fmaxf(ga[e], 0.f);
        return cvt4(t * ua);
    };
    if (TA) {
#pragma unroll
        for (int t = 0; t < 8; ++t) LDS(u32x2, ow + 16 * t * OST + j * 32) = hmul(acc[t][3], acc[t][4]);
        bar_lgkm();
        const unsigned oo = L_B + 16384 + tok * 128 + (((2 * j + (g >> 1)) ^ (tok & 7)) * 16) + (g & 1) * 8;
        const bool par = tok & 1;
        const int da = tok - (4 * g + (tok & 1));
        const bool ca = da >= 0, cb = da >= 2;
        const f32x4 z = {0, 0, 0, 0};
        s16x4 kprev = __builtin_bit_cast(s16x4, cvt4(acch[0]));
        u32x2 vprev = cvt4(acch[1]);
#pragma unroll
        for (int hb = 0; hb < 2; ++hb) {
            s16x4 kf[5], qf[4]; u32x2 vf[5];
            kf[0] = kprev; vf[0] = vprev;
#pragma unroll
            for (int i = 0; i < 4; ++i) {
                kf[i + 1] = __builtin_bit_cast(s16x4, cvt4(acc[4 * hb + i][1]));
                qf[i] = __builtin_bit_cast(s16x4, cvt4(acc[4 * hb + i][0]));
            }
            f32x4 sp[4], sc[4];
#pragma unroll
            for (int i = 0; i < 4; ++i) {
                sp[i] = __builtin_amdgcn_mfma_f32_16x16x16bf16_1k(kf[i], qf[i], z, 0, 0, 0);
                sc[i] = __builtin_amdgcn_mfma_f32_16x16x16bf16_1k(kf[i + 1], qf[i], z, 0, 0, 0);
            }
#pragma unroll
            for (int i = 0; i < 4; ++i) vf[i + 1] = cvt4(acc[4 * hb + i][2]);
            kprev = kf[4]; vprev = vf[4];
            unsigned lw[4];
#pragma unroll
            for (int i = 0; i < 4; ++i) {
                asm volatile("" : "+v"(sp[i]), "+v"(sc[i]));
                const float a0 = par ? sc[i][1] : sc[i][0], a1 = par ? sp[i][1] : sp[i][0];
                const float b0 = par ? sc[i][3] : sc[i][2], b1 = par ? sp[i][3] : sp[i][2];
                float la = ca ? a0 : a1, lb = cb ? b0 : b1;
                if (i == 0 && hb == 0) { if (p == 0) { la = ca ? la : -3.0e38f; lb = cb ? lb : -3.0e38f; } }
                lw[i] = pk2(la, lb);
            }
            tr4(lw);
            float e8[8];
#pragma unroll
            for (int i = 0; i < 4; ++i) { e8[2 * i] = bflo(lw[i]); e8[2 * i + 1] = bfhi(lw[i]); }
            const float mx = fmaxf(fmaxf(fmaxf(e8[0], e8[1]), fmaxf(e8[2], e8[3])), fmaxf(fmaxf(e8[4], e8[5]), fmaxf(e8[6], e8[7])));
            float sum = 0;
#pragma unroll
            for (int i = 0; i < 8; ++i) { e8[i] = __builtin_amdgcn_exp2f((e8[i] - mx) * (0.25f * LOG2E)); sum += e8[i]; }
            const float inv = __builtin_amdgcn_rcpf(sum);
#pragma unroll
            for (int i = 0; i < 4; ++i) lw[i] = pk2(e8[2 * i] * inv, e8[2 * i + 1] * inv);
            tr4(lw);
#pragma unroll
            for (int i = 0; i < 4; ++i) {
                const unsigned pw = lw[i];
                const unsigned wa = par ? (pw << 16) : (pw & 0xffffu), wb = par ? (pw & 0xffff0000u) : (pw >> 16);
                const u32x4 pp = {ca ? 0u : wa, cb ? 0u : wb, ca ? wa : 0u, cb ? wb : 0u};
                const u32x4 vv = {vf[i][0], vf[i][1], vf[i + 1][0], vf[i + 1][1]};
                sp[i] = mfma32(__builtin_bit_cast(bf16x8, vv), __builtin_bit_cast(bf16x8, pp), z);
            }
#pragma unroll
            for (int i = 0; i < 4; ++i) LDS(u32x2, oo + ((4 * hb + i) >> 1) * BBLK + (i & 1) * 2048) = cvt4(sp[i]);
        }
    } else {
        const int jj = j0 - 8, odd = jj & 1;
        const int lo_off = (odd ? 64 : 0) + j * 64;
        const int mx_off = (odd ? 0 : 256) + j * 16;
#pragma unroll
        for (int i = 0; i < 8; ++i)
#pragma unroll
            for (int c = 0; c < 2; ++c) LDS(u32x2, ow + 16 * i * OST + lo_off + c * 32) = hmul(acc[i][c], acc[i][c + 2]);
#pragma unroll
        for (int i = 0; i < 8; i += 2) {
            f32x4 ga, ua;
#pragma unroll
            for (int e = 0; e < 4; ++e) {
                unsigned a = __float_as_uint(acc[i][4][e]), b = __float_as_uint(acc[i + 1][4][e]);
                swap32(a, b);
                ga[e] = __uint_as_float(a); ua[e] = __uint_as_float(b);
            }
            LDS(u32x2, L_A + (16 * (i + (g >> 1)) + tok) * OST + mx_off + (g & 1) * 8) = hmul(ga, ua);
        }
        store_h(a2, false, p, j0);
    }
}

constexpr int cnt_iv(int t) { return t < 12 ? ((t & 1) ? 7 : 8) : (t == 12 ? 9 : 4); }
constexpr int wait_iv(int t) {
    return t < 2 ? 15 : (t == 14 ? 4 : 0) + cnt_iv(t - 1) + cnt_iv(t);
}

__global__ __launch_bounds__(512, 2) void kfused(const u16* x, const u16* wqkv, const u16* wo, const u16* wg, const u16* wu, const u16* wd,
                                                 u16* a2, u16* out, unsigned* flags) {
    const int w = __builtin_amdgcn_readfirstlane(threadIdx.x >> 6);
    const int b = blockIdx.x, xcd = b & 7, loc = b >> 3;
    if (w == 7) {
        unsigned long long pc;
        asm volatile("s_getpc_b64 %0" : "=s"(pc));
        const int l = threadIdx.x & 63;
        int ln = (l < 32) ? l : 32 + loc * 7 + ((l - 32) % 7);
        ln = ln < CODE_LINES ? ln : 0;
        asm volatile("s_mov_b32 m0, %0\n\ts_nop 0" :: "s"(L_DUMP));
        touch_s((const char*)(pc & ~127ull), (unsigned)(ln * 128));
    }
    const int ci = loc >> 3;
    const int p = (xcd & 1) * 8 + (loc & 7), qd = xcd >> 1;
    const int j0 = (ci < 2) ? 2 * qd + ci : 6 + 2 * qd + ci;
    const int rt = (xcd & 1) * 16 + (xcd >> 1) * 4 + (loc & 3), ct = loc >> 2;
    const int R0 = rt * 64, C0 = ct * 64;
    const bool TA = ci < 2;
    u16* const xn = a2 + (5 << 20);
    unsigned* const inbox = flags + 16384;
    ld_row(L_A + w * 1024, x, p, 16 + 8 * j0 + w);
    if (w == 0) ld_row(L_A + 8192, x, p, j0);
    if (w == 3) touch_v_sc1(inbox + (p * 16 + j0) * 32 + (threadIdx.x & 31), L_FP);
    if (threadIdx.x == 0) LDS(volatile u32x4, L_CNT) = u32x4{0u, 0u, 0u, 0u};
    bar_lgkm();

    const int h = w & 3;
    const int l = threadIdx.x & 63, kq = l >> 3;
    const int lchB = (l & 7) ^ ((((kq >> 1) & 1) | ((h & 1) << 1)) << 1);
    const char *sB0, *sB1, *sB3, *sB4, *gM; unsigned vB0, vB1, st0;
    {
        const int k = 8 * h + kq;
        const int jj = j0 - 8, f0 = 512 + 160 * jj, odd = jj & 1;
        st0 = TA ? 3072u : (unsigned)(F_ * 2);
        vB1 = (unsigned)(k * (F_ * 2) + lchB * 16);
        vB0 = (unsigned)(k * st0 + lchB * 16);
        sB0 = TA ? (const char*)(wqkv + 64 * j0) : (const char*)(wg + f0 + (odd ? 32 : 0));
        sB1 = sB0 + (TA ? 1024 : 128);
        sB3 = TA ? (const char*)(wg + 64 * j0) : (const char*)(wu + f0 + (odd ? 32 : 0));
        sB4 = TA ? (const char*)(wu + 64 * j0) : sB3 + 128;
        const char* m0_ = (const char*)(wg + f0 + (odd ? 0 : 128)), *m1_ = (const char*)(wu + f0 + (odd ? 0 : 128)) - 64;
        gM = (TA ? sB0 + 2048 : (lchB < 4 ? m0_ : m1_)) + vB0;
    }
    const unsigned dB = L_B + h * 1024;
    auto issB = [&](auto SLOT) {
        constexpr int so = SLOT * BBLK;
        setm0<so>(dB); nop1(); dma_s<0>(sB0, vB0);
        setm0<so + 4096>(dB); nop1(); dma_s<0>(sB1, vB0);
        setm0<so + 8192>(dB); nop1(); dma_v(gM);
        setm0<so + 12288>(dB); nop1(); dma_s<0>(sB3, vB1);
        setm0<so + 16384>(dB); nop1(); dma_s<0>(sB4, vB1);
        vB1 += 32 * F_ * 2; vB0 += 32 * st0; gM += 32 * st0;
    };
    if (w >= 4) issB(IC(0));
    ln_exchange(xn, inbox, w, p, j0);

    if (w >= 4) {
        issB(IC(1));
        wait_vm<2>();
        issB(IC(2));
        unsigned* const myP = flags + p * 32 + j0;
        const unsigned* const myE = flags + 512 + (p * 16 + j0) * 32 + (l & 15);
        const unsigned* const pollP = flags + (rt >> 1) * 32 + (l & 15);
        unsigned* const echoE = flags + 512 + ((rt >> 1) * 16 + (l & 15)) * 32 + (rt & 1) * 8 + ct;
        unsigned myv = 0, last = 2;
        const char* xs = (const char*)(xn + (size_t)p * 144 * 512);
        unsigned vA[4], vH;
        {
            const int rr = 8 * h + kq;
            vA[0] = (unsigned)((16 + rr) * 1024 + (((l & 7) ^ ((rr >> 1) & 7)) * 16));
            vA[1] = vA[0] + 32768; vA[2] = vA[0] + 65536; vA[3] = vA[0] + 98304;
            const int hr = 4 * h + (kq & 3);
            vH = (unsigned)(hr * 1024 + (((l & 7) ^ ((hr >> 1) & 7)) * 16));
        }
        const unsigned dA = L_A + 2048 + h * 1024, dH = L_A + h * 512;
        auto issA = [&](auto BLK, auto HALF) {
            constexpr int S = BLK, hf = HALF, so = (S % NA) * ABLK - S * 128;
            setm0<so + hf * 8192>(dA); nop1(); dma_s_sc1<S * 128>(xs, vA[2 * hf]);
            setm0<so + hf * 8192 + 4096>(dA); nop1(); dma_s_sc1<S * 128>(xs, vA[2 * hf + 1]);
            if constexpr (hf == 0) { setm0<so>(dH); dma_s32_sc1<S * 128>(xs, vH); }
        };
        const unsigned vW = (unsigned)((8 * h + kq) * 1024 + lchB * 16);
        const char* const wdc = (const char*)(wd + C0);
        const char* const woc = (const char*)(wo + C0);
        auto issW = [&](auto ST) {
            constexpr int st = ST, so = ((st + 1) % NB) * BBLK;
            const char* base = (st < 14) ? wdc + st * 131072 : woc + (st - 14) * 131072;
            sfor<4>([&](auto C) { constexpr int c = C; setm0<so + c * 4096>(dB); nop1(); dma_s<0>(base + c * 32768, vW); });
        };
        auto warm1 = [&](auto T) {
            constexpr int t = T;
            if (h == (t & 3)) {
                const int row = 224 + 36 * (loc & 7) + (l < 36 ? l : 35);
                const char* a;
                if (TA) a = (t < 3) ? (const char*)(wqkv + 512 * t + 64 * j0) + row * 3072 : (const char*)((t & 1 ? wg : wu) + 64 * j0) + row * (F_ * 2);
                else a = (const char*)((t & 1 ? wg : wu) + ((512 + 160 * (j0 - 8)) & ~63) + 64 * (t >> 1)) + row * (F_ * 2);
                touch_v(a, L_DUMP);
            }
        };
        auto warm2 = [&](auto R) {
            constexpr int r = R;
            if (h == ((r + 1) & 3)) {
                const int vr = (r * 32 + loc) * 8;
                const char* a = (r < 7) ? (const char*)wd + vr * 1024 : (const char*)wo + (vr - 1792) * 1024;
                touch_v(a + l * 128, L_DUMP);
            }
        };
        bar();
        issA(IC(0), IC(0)); issA(IC(0), IC(1));
        issA(IC(1), IC(0)); issA(IC(1), IC(1));
        wait_bar<5>();
        if (h == 0) touch_v_sc1(myE, L_FE);
        sfor<16>([&](auto T) {
            constexpr int t = T;
            if constexpr (t < 12) issA(IC(t / 2 + 2), IC(t & 1));
            if constexpr (t < 13) issB(IC((t + 3) % NB));
            if constexpr (t >= 12) issW(IC(t - 12));
            if constexpr (t < 6) warm1(T);
            if constexpr (t >= 6 && t < 15) warm2(IC(t - 6));
            if constexpr (t == 2) {
                if (h == 0) {
                    const unsigned ev = LDS(const volatile unsigned, L_FE + l * 4);
                    unsigned v = 0;
                    while (__ballot(ev == v) != 0ull) v += 4;
                    myv = v;
                    st_sc1(myP, v, 1ull);
                }
            }
            if constexpr (t == 6) { if (h == 0) touch_v_sc1(pollP, L_FP); }
            if constexpr (t == 10) {
                if (h == 0) {
                    const unsigned pv = LDS(const volatile unsigned, L_FP + l * 4);
                    st_sc1(echoE, pv, __ballot(!(pv & 3)) & 0xffffull);
                    last = (pv & 3) ? last : pv;
                }
            }
            if constexpr (t == 14) { if (h == 0) touch_v_sc1(myE, L_FE); }
            if constexpr (t < 15) wait_bar<wait_iv(t)>(); else bar();
        });
        issW(IC(4));
        unsigned vA2[2];
        { const int row = 8 * h + kq; vA2[0] = (unsigned)(row * (A2LD * 2) + (((l & 7) ^ ((row >> 1) & 7)) * 16)); vA2[1] = vA2[0] + 32 * A2LD * 2; }
        const char* const sa = (const char*)(a2 + (size_t)R0 * A2LD);
        const unsigned dA2 = L_A + h * 1024;
        const char* tA;
        { int id = h * 40 + (l < 40 ? l : 0); id += ct * 160; tA = sa + (size_t)(id & 63) * (A2LD * 2) + 2048 + (id >> 6) * 128; }
        store_h(a2, TA, p, j0);
        unsigned ev = 0, state = 0, shown = 0;
        auto serve = [&](unsigned pv, unsigned long long mask, unsigned ign, bool flush) {
#pragma unroll 1
            for (unsigned spin = 0; spin < SPIN_CAP; ++spin) {
                if (state == 0 && __builtin_amdgcn_readfirstlane(LDS(const volatile unsigned, L_CNT + 4)) == (TA ? 4u : 8u)) state = TA ? 1u : 3u;
                if (state != shown && __ballot(ev != myv) == 0ull) { st_sc1(myP, myv + state, 1ull); shown = state; }
                st_sc1(echoE, pv, __ballot(!(pv & 3) && pv != last) & 0xffffull);
                last = (pv & 3) ? last : pv;
                if ((__ballot(((pv - last) | ign) != 3u || (last & 3)) & mask) == 0ull && !(flush && shown != 3u)) break;
                ld2_sc1(pv, pollP, ev, myE);
            }
        };
        if (h == 0) {
            ev = LDS(const volatile unsigned, L_FE + l * 4);
            serve(last, 0xffull, 2u, false);
            if (l == 0) LDS(volatile unsigned, L_CNT + 8) = 1u;
        }
        lds_wait(L_CNT + 12, TA ? 4u : 8u);
        lds_wait(L_CNT + 8, 1u);
        auto issA2 = [&](auto ST) {
            constexpr int st = ST, ko = (st < 14) ? 1024 + st * 256 : (st - 14) * 256, so = (st % NA) * ABLK;
            const char* sk = sa + ko;
            setm0<so>(dA2); nop1(); dma_s_sc1<0>(sk, vA2[0]);
            setm0<so + 4096>(dA2); nop1(); dma_s_sc1<0>(sk, vA2[1]);
            setm0<so + 8192 - 128>(dA2); nop1(); dma_s_sc1<128>(sk, vA2[0]);
            setm0<so + 12288 - 128>(dA2); nop1(); dma_s_sc1<128>(sk, vA2[1]);
        };
        issA2(IC(0)); issA2(IC(1));
        if (h == 0) serve(last, 0ull, 0u, false);
        wait_vm<4>();
        bar();
        if (h == 0) { serve(last, 0ull, 0u, false); touch_v_sc1(pollP, L_FP); }
        if (TA) {
            const __amdgpu_buffer_rsrc_t a2r = __builtin_amdgcn_make_buffer_rsrc(a2, 0, S_ * A2LD * 2, 0x00020000);
            const int t4 = threadIdx.x & 255, r_ = t4 >> 3;
            const unsigned rd = L_B + 16384 + r_ * 128 + (((t4 & 7) ^ (r_ & 7)) * 16);
            const unsigned go = ((p * 128 + r_) * A2LD + 64 * j0 + (t4 & 7) * 8) * 2;
            u32x4 v[4];
#pragma unroll
            for (int it = 0; it < 4; ++it) v[it] = LDS(const u32x4, rd + it * BBLK);
#pragma unroll
            for (int it = 0; it < 4; ++it) __builtin_amdgcn_raw_buffer_store_b128(v[it], a2r, go, it * 32 * A2LD * 2, 16);
        }
        issA2(IC(2));
        if (TA) wait_vm<8>(); else wait_vm<4>();
        bar();
        sfor<16>([&](auto ST) {
            constexpr int st = ST;
            if constexpr (st < 13) issW(IC(st + 5));
            if constexpr (st < 15) issA2(IC(st + 3));
            if constexpr (st == 0) { if (h == 0) serve(LDS(const volatile unsigned, L_FP + l * 4), 0xff00ull, 2u, false); }
            if constexpr (st == 1) touch_v_sc1(tA, L_DUMP);
            if constexpr (st == 2) { if (h == 0) { state = 3u; serve(last, 0ull, 0u, false); } }
            if constexpr (st == 7) { if (h == 0) touch_v_sc1(pollP, L_FP); }
            if constexpr (st == 10) { if (h == 0) serve(LDS(const volatile unsigned, L_FP + l * 4), 0xffull, 0u, true); }
            wait_bar<(st < 13) ? 8 : (st < 15) ? 4 : 0>();
        });
        return;
    }
    if (w < 3) bar();
    if (TA) p1_mfma<true>(a2, p, j0); else p1_mfma<false>(a2, p, j0);
    {
        int l2 = threadIdx.x & 63;
        asm volatile("" : "+v"(l2));
        const int g = l2 >> 4, tok = l2 & 15;
        const int wm = w >> 1, wn = w & 1;
        const unsigned a2A = L_A + (32 * wm + tok) * 128 + ((g ^ (tok >> 1)) * 16);
        const unsigned a2B = L_B + (8 * g + (tok >> 2)) * 128 + (((2 * wn) ^ (((tok >> 3) & 1) | ((g & 1) << 1))) * 32) + (l2 & 3) * 8;
        const size_t orow = (size_t)(R0 + 32 * wm + tok) * D_ + C0 + 32 * wn + 4 * g;
        u32x2 xv[2][2];
#pragma unroll
        for (int i = 0; i < 2; ++i)
#pragma unroll
            for (int c = 0; c < 2; ++c) xv[i][c] = *(const u32x2*)(x + orow + i * 16 * D_ + 16 * c);
        f32x4 c2[2][2][2];
#pragma unroll
        for (int a = 0; a < 2; ++a)
#pragma unroll
            for (int i = 0; i < 2; ++i)
#pragma unroll
                for (int c = 0; c < 2; ++c) c2[a][i][c] = f32x4{0, 0, 0, 0};
        bf16x8 ga[2][4][2], gb[2][4][2];
        unsigned b2o = BBLK, a2o = 0;
        unsigned pa0 = a2A, pa1 = pa0 ^ 64, pb0 = a2B + b2o, pb1 = pb0 ^ 32;
        auto rd = [&](auto SET, auto I) {
            constexpr int set = SET, i = I, ks = i >> 1;
            ga[set][ks][i & 1] = lds_a(((ks & 1) ? pa1 : pa0) + (ks >> 1) * 8192 + (i & 1) * 2048);
            gb[set][ks][i & 1] = lds_b(((i & 1) ? pb1 : pb0) + ks * 4096);
        };
        auto adv = [&]() {
            b2o = (b2o == (NB - 1) * BBLK) ? 0u : b2o + BBLK;
            a2o = (a2o == (NA - 1) * ABLK) ? 0u : a2o + ABLK;
            pa0 = a2A + a2o; pa1 = pa0 ^ 64; pb0 = a2B + b2o; pb1 = pb0 ^ 32;
            asm volatile("" : "+v"(pa0), "+v"(pa1), "+v"(pb0), "+v"(pb1));
        };
        auto stage = [&](auto SET, auto ACC, auto LAST) {
            constexpr int set = SET, ac = ACC; constexpr bool lastst = LAST;
            if constexpr (!lastst) bar_lgkm();
            SB();
            sfor<16>([&](auto I) {
                constexpr int idx = I, ks = idx >> 2, i = (idx >> 1) & 1, c = idx & 1;
                c2[ac][i][c] = mfma32(gb[set][ks][c], ga[set][ks][i], c2[ac][i][c]);
                if constexpr (!lastst && idx < 8) rd(IC(set ^ 1), I);
                if constexpr (!lastst && idx == 10) adv();
                SB();
            });
        };
        typedef std::integral_constant<bool, true> T_; typedef std::integral_constant<bool, false> F_t;
        asm volatile("s_waitcnt vmcnt(0)" : "+v"(xv[0][0]), "+v"(xv[0][1]), "+v"(xv[1][0]), "+v"(xv[1][1]) :: "memory");
        bar_lgkm();
        sfor<8>([&](auto I) { rd(IC(0), I); });
        adv();
#pragma unroll 1
        for (int it = 0; it < 7; ++it) {
            stage(IC(0), IC(1), F_t{}); stage(IC(1), IC(1), F_t{});
        }
        stage(IC(0), IC(0), F_t{}); stage(IC(1), IC(0), F_t{});
        stage(IC(0), IC(0), F_t{}); stage(IC(1), IC(0), T_{});
#pragma unroll
        for (int i = 0; i < 2; ++i)
#pragma unroll
            for (int c = 0; c < 2; ++c) {
                float r[4];
#pragma unroll
                for (int e = 0; e < 4; ++e) {
                    const float d = rbf(c2[1][i][c][e]);
                    const float sg = rbf(d * __builtin_amdgcn_rcpf(1.f + __builtin_amdgcn_exp2f(-d * LOG2E)));
                    const float t = rbf(rbf(c2[0][i][c][e]) * sg);
                    const float xx = (e & 1) ? bfhi(xv[i][c][e >> 1]) : bflo(xv[i][c][e >> 1]);
                    r[e] = xx + t;
                }
                *(u32x2*)(out + orow + i * 16 * D_ + 16 * c) = u32x2{pk2(r[0], r[1]), pk2(r[2], r[3])};
            }
    }
}

extern "C" void kernel_launch(void* const* d_in, const int* in_sizes, int n_in,
                              void* d_out, int out_size, void* d_ws, size_t ws_size,
                              hipStream_t stream) {
    (void)hipFuncSetAttribute((const void*)kfused, hipFuncAttributeMaxDynamicSharedMemorySize, K_LDS);
    kfused<<<256, 512, K_LDS, stream>>>((const u16*)d_in[0], (const u16*)d_in[1], (const u16*)d_in[2], (const u16*)d_in[4], (const u16*)d_in[5],
                                        (const u16*)d_in[6], (u16*)d_ws, (u16*)d_out, (unsigned*)((char*)d_ws + (16 << 20)));
}
```

```cpp
#include <hip/hip_runtime.h>
#include <stdint.h>
#include <utility>

typedef unsigned short u16;
typedef __attribute__((ext_vector_type(8))) __bf16 bf16x8;
typedef __attribute__((ext_vector_type(2))) __bf16 bf16x2;
typedef __attribute__((ext_vector_type(4))) short s16x4;
typedef __attribute__((ext_vector_type(8))) short s16x8;
typedef __attribute__((ext_vector_type(4))) float f32x4;
typedef __attribute__((ext_vector_type(2))) float f32x2;
typedef __attribute__((ext_vector_type(4))) unsigned u32x4;
typedef __attribute__((ext_vector_type(2))) unsigned u32x2;
#define LAS __attribute__((address_space(3)))
#define SB() __builtin_amdgcn_sched_barrier(0)
#define IC(n) std::integral_constant<int, (n)>{}
#define LDS(T, a) (*(LAS T*)(uintptr_t)(a))

constexpr int S_ = 2048, D_ = 512, F_ = 1792;
constexpr int A2LD = 2304;
constexpr float LOG2E = 1.4426950408889634f;

template <int... I, class Fn> __device__ __forceinline__ void sfor_impl(std::integer_sequence<int, I...>, Fn&& f) { (f(std::integral_constant<int, I>{}), ...); }
template <int N, class Fn> __device__ __forceinline__ void sfor(Fn&& f) { sfor_impl(std::make_integer_sequence<int, N>{}, f); }

__device__ __forceinline__ float rbf(float f) { return (float)(__bf16)f; }
__device__ __forceinline__ unsigned pk2(float lo, float hi) {
    bf16x2 v; v[0] = (__bf16)lo; v[1] = (__bf16)hi; return __builtin_bit_cast(unsigned, v);
}
__device__ __forceinline__ float bflo(unsigned u) { return __uint_as_float(u << 16); }
__device__ __forceinline__ float bfhi(unsigned u) { return __uint_as_float(u & 0xffff0000u); }

template <int OFF> __device__ __forceinline__ void setm0(unsigned base) {
    asm volatile("s_add_u32 m0, %0, %1" :: "s"(base), "n"(OFF) : "scc");
}
__device__ __forceinline__ void dma_v(const void* g) { asm volatile("global_load_lds_dwordx4 %0, off" :: "v"(g) : "memory"); }
__device__ __forceinline__ void dma_vm(const void* g, unsigned long long mask) {
    asm volatile("s_mov_b64 exec, %1\n\tglobal_load_lds_dwordx4 %0, off\n\ts_mov_b64 exec, -1" :: "v"(g), "s"(mask) : "memory");
}
template <int IO> __device__ __forceinline__ void dma_s(const void* sb, unsigned vo) {
    asm volatile("global_load_lds_dwordx4 %0, %1 offset:%2" :: "v"(vo), "s"(sb), "n"(IO) : "memory");
}
template <int IO> __device__ __forceinline__ void dma_s_sc1(const void* sb, unsigned vo) {
    asm volatile("global_load_lds_dwordx4 %0, %1 offset:%2 sc1" :: "v"(vo), "s"(sb), "n"(IO) : "memory");
}
template <int LIM> __device__ __forceinline__ void dma_s_c(int cnt, const void* sb, unsigned vo) {
    asm volatile("s_cmp_ge_i32 %2, %3\n\ts_cbranch_scc1 1f\n\tglobal_load_lds_dwordx4 %0, %1\n1:" :: "v"(vo), "s"(sb), "s"(cnt), "n"(LIM) : "memory", "scc");
}
template <int IO> __device__ __forceinline__ void dma_s32_sc1(const void* sb, unsigned vo) {
    asm volatile("s_mov_b64 exec, %3\n\tglobal_load_lds_dwordx4 %0, %1 offset:%2 sc1\n\ts_mov_b64 exec, -1" :: "v"(vo), "s"(sb), "n"(IO), "s"(0xffffffffull) : "memory");
}
__device__ __forceinline__ void touch_s(const void* sb, unsigned vo) {
    asm volatile("global_load_lds_dword %0, %1" :: "v"(vo), "s"(sb) : "memory");
}
__device__ __forceinline__ unsigned ld_sc1(const unsigned* p) {
    unsigned a; asm volatile("global_load_dword %0, %1, off sc1\n\ts_waitcnt vmcnt(0)" : "=&v"(a) : "v"(p) : "memory"); return a;
}
template <int LIM> __device__ __forceinline__ void dma_s16_c(int cnt, const void* sb, unsigned vo) {
    asm volatile("s_cmp_ge_i32 %2, %3\n\ts_cbranch_scc1 1f\n\ts_mov_b64 exec, 0xffff\n\tglobal_load_lds_dwordx4 %0, %1\n\ts_mov_b64 exec, -1\n1:"
                 :: "v"(vo), "s"(sb), "s"(cnt), "n"(LIM) : "memory", "scc");
}
__device__ __forceinline__ void nop1() { asm volatile("s_nop 0"); }
__device__ __forceinline__ void touch_v(const void* gaddr, unsigned ldsaddr) {
    asm volatile("s_mov_b32 m0, %1\n\ts_nop 0\n\tglobal_load_lds_dword %0, off" :: "v"(gaddr), "s"(ldsaddr) : "memory");
}
__device__ __forceinline__ void touch_v_sc1(const void* gaddr, unsigned ldsaddr) {
    asm volatile("s_mov_b32 m0, %1\n\ts_nop 0\n\tglobal_load_lds_dword %0, off sc1" :: "v"(gaddr), "s"(ldsaddr) : "memory");
}
template <int N> __device__ __forceinline__ void wait_vm() { asm volatile("s_waitcnt vmcnt(%0)" :: "n"(N) : "memory"); }
template <int N> __device__ __forceinline__ void wait_bar() {
    __builtin_amdgcn_s_waitcnt(0xC07F);
    asm volatile("s_waitcnt vmcnt(%0)\n\ts_barrier" :: "n"(N) : "memory");
}
__device__ __forceinline__ void bar_lgkm() { __builtin_amdgcn_s_waitcnt(0xC07F); asm volatile("s_barrier" ::: "memory"); }
__device__ __forceinline__ void bar() { asm volatile("s_barrier" ::: "memory"); }
__device__ __forceinline__ void ld2_sc1(unsigned& a, const unsigned* pa, unsigned& b, const unsigned* pb) {
    asm volatile("global_load_dword %0, %2, off sc1\n\tglobal_load_dword %1, %3, off sc1\n\ts_waitcnt vmcnt(0)"
                 : "=&v"(a), "=&v"(b) : "v"(pa), "v"(pb) : "memory");
}
__device__ __forceinline__ void st_sc1(unsigned* p, unsigned v, unsigned long long mask) {
    asm volatile("s_mov_b64 exec, %2\n\ts_cbranch_execz 1f\n\tglobal_store_dword %0, %1, off sc1\n1:\n\ts_mov_b64 exec, -1"
                 :: "v"(p), "v"(v), "s"(mask) : "memory");
}

__device__ __forceinline__ bf16x8 lds_a(unsigned addr) { return LDS(const bf16x8, addr); }
__device__ __forceinline__ bf16x8 lds_b(unsigned addr) {
    s16x4 lo = __builtin_amdgcn_ds_read_tr16_b64_v4i16((LAS s16x4*)(uintptr_t)addr);
    s16x4 hi = __builtin_amdgcn_ds_read_tr16_b64_v4i16((LAS s16x4*)(uintptr_t)(addr + 512));
    s16x8 r = __builtin_shufflevector(lo, hi, 0, 1, 2, 3, 4, 5, 6, 7);
    return __builtin_bit_cast(bf16x8, r);
}
__device__ __forceinline__ f32x4 mfma32(bf16x8 a, bf16x8 b, f32x4 c) { return __builtin_amdgcn_mfma_f32_16x16x32_bf16(a, b, c, 0, 0, 0); }
__device__ __forceinline__ void swap16(unsigned& a, unsigned& b) { auto r = __builtin_amdgcn_permlane16_swap(a, b, false, false); a = r[0]; b = r[1]; }
__device__ __forceinline__ void swap32(unsigned& a, unsigned& b) { auto r = __builtin_amdgcn_permlane32_swap(a, b, false, false); a = r[0]; b = r[1]; }
__device__ __forceinline__ void tr4(unsigned (&v)[4]) { swap16(v[0], v[1]); swap16(v[2], v[3]); swap32(v[0], v[2]); swap32(v[1], v[3]); }
__device__ __forceinline__ float xadd16(float v) { unsigned a = __float_as_uint(v), b = a; swap16(a, b); return __uint_as_float(a) + __uint_as_float(b); }
template <int CTRL> __device__ __forceinline__ float dadd(float v) {
    return v + __int_as_float(__builtin_amdgcn_update_dpp(0, __float_as_int(v), CTRL, 0xf, 0xf, true));
}
__device__ __forceinline__ float xadd32(float v) { unsigned a = __float_as_uint(v), b = a; swap32(a, b); return __uint_as_float(a) + __uint_as_float(b); }
__device__ __forceinline__ float red8(float v) { return dadd<0x141>(dadd<0x4E>(dadd<0xB1>(v))); }
__device__ __forceinline__ float red64(float v) { return xadd32(xadd16(dadd<0x140>(red8(v)))); }

constexpr int NB = 5, NA = 3;
constexpr int BBLK = 20480, ABLK = 18432;
constexpr int L_B = 0, L_A = NB * BBLK, L_ST = L_A + NA * ABLK;
constexpr int L_CNT = L_ST;
constexpr int L_DUMP = L_ST + 64;
constexpr int L_FE = L_DUMP + 256, L_FP = L_FE + 256;
constexpr int K_LDS = L_FP + 256;
constexpr unsigned SPIN_CAP = 1u << 16;
#define WARM_EARLY 1
#define W2_AT 4
#define CODE_LINES 236
#define TOUCH_AT 2

__device__ __forceinline__ void ld_row(unsigned dst, const u16* x, int p, int ir) {
    int gr = p * 128 - 16 + ir; gr = gr < 0 ? 0 : gr;
    setm0<0>(dst); nop1(); dma_s<0>(x + (size_t)gr * 512, (threadIdx.x & 63) * 16);
}
__device__ __forceinline__ void ln_row(u32x4 v, u16* xn, int p, int ir, bool real) {
    float f[8], s = 0;
#pragma unroll
    for (int e = 0; e < 4; ++e) { const unsigned u = v[e]; f[2 * e] = bflo(u); f[2 * e + 1] = bfhi(u); s += f[2 * e] + f[2 * e + 1]; }
    const float sh = __int_as_float(__builtin_amdgcn_readfirstlane(__float_as_int(f[0])));
    float q = 0;
#pragma unroll
    for (int e = 0; e < 8; ++e) { const float d = f[e] - sh; q = fmaf(d, d, q); }
    const float mean = red64(s) * (1.f / 512.f);
    const float dm = mean - sh;
    const float var = fmaxf(red64(q) * (1.f / 512.f) - dm * dm, 0.f);
    const float sd = rbf(__builtin_amdgcn_sqrtf(rbf(rbf(var) + 1e-5f)));
    const float mb = rbf(mean), rc = __builtin_amdgcn_rcpf(sd);
    float o[8];
#pragma unroll
    for (int e = 0; e < 8; ++e) {
        const float a = rbf(f[e] - mb);
        const float q0 = a * rc;
        o[e] = fmaf(fmaf(-q0, sd, a), rc, q0);
    }
    const u32x4 ov = {pk2(o[0], o[1]), pk2(o[2], o[3]), pk2(o[4], o[5]), pk2(o[6], o[7])};
    const __amdgpu_buffer_rsrc_t xr = __builtin_amdgcn_make_buffer_rsrc(xn, 0, 16 * 144 * 1024, 0x00020000);
    if (real) __builtin_amdgcn_raw_buffer_store_b128(ov, xr, (unsigned)((p * 144 + ir) * 1024 + (threadIdx.x & 63) * 16), 0, 16);
    else asm volatile("" :: "v"(ov));
}
__device__ __forceinline__ void ln_done() {
    wait_vm<0>();
    if ((threadIdx.x & 63) == 0) __hip_atomic_fetch_add((LAS unsigned*)(uintptr_t)L_CNT, 1u, __ATOMIC_RELAXED, __HIP_MEMORY_SCOPE_WORKGROUP);
}

__device__ __forceinline__ void ln_exchange(u16* xn, unsigned* inbox, int w, int p, int j0) {
    const int l = threadIdx.x & 63;
    const unsigned* in = inbox + (p * 16 + j0) * 32 + (l & 31);
    unsigned* outp = inbox + (p * 16 + (l & 15)) * 32 + ((l & 16) ? j0 : 16 + j0);
    unsigned want = 0;
    const int npass = (w == 0) ? 3 : 2;
#pragma unroll 1
    for (int pass = 1; pass < npass; ++pass) {
        if (pass == 1) {
            if (w < 4) wait_vm<0>(); else wait_vm<5>();
            if (w == 3) {
                want = LDS(const volatile unsigned, L_FP + l * 4) + 1u;
                st_sc1(outp, want, 0xffff0000ull);
            }
        }
        const u32x4 v = LDS(const volatile u32x4, L_A + (pass == 2 ? 8 : w) * 1024 + l * 16);
        ln_row(v, xn, p, pass == 2 ? j0 : 16 + 8 * j0 + w, pass != 0);
    }
    ln_done();
    if (w != 3) return;
    unsigned u = ld_sc1(in);
    unsigned spin = 0;
#pragma unroll 1
    while (__builtin_amdgcn_readfirstlane(LDS(const volatile unsigned, L_CNT)) != 8u && ++spin < (SPIN_CAP << 4)) {}
    st_sc1(outp, u, 0xffffull);
    bool go = false;
#pragma unroll 1
    for (spin = 0; spin < SPIN_CAP; ++spin) {
        const unsigned echoed = u;
        u = ld_sc1(in);
        st_sc1(outp, u, __ballot(u != echoed) & 0xffffull);
        if (go) break;
        if ((__ballot(u != want) & 0xffff0000ull) == 0ull) { go = true; bar(); }
    }
    if (!go) bar();
}
__device__ __forceinline__ void store_h(u16* a2, bool TA, int p, int j0) {
    int tid = threadIdx.x;
    asm volatile("" : "+v"(tid));
    const __amdgpu_buffer_rsrc_t a2r = __builtin_amdgcn_make_buffer_rsrc(a2, 0, S_ * A2LD * 2, 0x00020000);
    if (TA) {
        const int t4 = tid & 255;
        const unsigned rd = L_A + (t4 >> 3) * 144 + (t4 & 7) * 16;
        const unsigned go = ((p * 128 + (t4 >> 3)) * A2LD + 512 + 64 * j0 + (t4 & 7) * 8) * 2;
        bar_lgkm();
        u32x4 v[4];
#pragma unroll
        for (int it = 0; it < 4; ++it) v[it] = LDS(const u32x4, rd + it * 32 * 144);
#pragma unroll
        for (int it = 0; it < 4; ++it) __builtin_amdgcn_raw_buffer_store_b128(v[it], a2r, go, it * 32 * A2LD * 2, 16);
    } else {
        unsigned rd[5], go[5];
#pragma unroll
        for (int it = 0; it < 5; ++it) {
            const int id = it * 512 + tid, r_ = id / 20, c_ = id - r_ * 20;
            rd[it] = L_A + r_ * 336 + c_ * 16;
            go[it] = ((p * 128 + r_) * A2LD + 1024 + 160 * (j0 - 8) + c_ * 8) * 2;
        }
        bar_lgkm();
        u32x4 v[5];
#pragma unroll
        for (int it = 0; it < 5; ++it) v[it] = LDS(const u32x4, rd[it]);
#pragma unroll
        for (int it = 0; it < 5; ++it) __builtin_amdgcn_raw_buffer_store_b128(v[it], a2r, go[it], 0, 16);
    }
    if ((tid & 63) == 0) __hip_atomic_fetch_add((LAS unsigned*)(uintptr_t)(L_CNT + 12), 1u, __ATOMIC_RELAXED, __HIP_MEMORY_SCOPE_WORKGROUP);
    wait_vm<0>();
    if ((tid & 63) == 0) __hip_atomic_fetch_add((LAS unsigned*)(uintptr_t)(L_CNT + 4), 1u, __ATOMIC_RELAXED, __HIP_MEMORY_SCOPE_WORKGROUP);
}
__device__ __forceinline__ void lds_wait(int addr, unsigned val) {
    unsigned spin = 0;
#pragma unroll 1
    while (__builtin_amdgcn_readfirstlane(LDS(const volatile unsigned, addr)) != val && ++spin < (SPIN_CAP << 6)) {}
}

template <bool TA>
__device__ __forceinline__ void p1_mfma(u16* a2, int p, int j0) {
    int l = threadIdx.x & 63;
    asm volatile("" : "+v"(l));
    const int j = __builtin_amdgcn_readfirstlane(threadIdx.x >> 6);
    const int g = l >> 4, tok = l & 15;
    const unsigned adA0 = L_A + tok * 128 + ((g ^ (tok >> 1)) * 16);
    const int f2 = ((tok >> 3) & 1) | ((g & 1) << 1);
    const unsigned comB = L_B + (8 * g + (tok >> 2)) * 128;
    unsigned adB0, adB1 = 0;
    {
        const int pp = l & 3;
        if (TA) adB0 = comB + ((j ^ f2) * 32) + pp * 8;
        else {
            adB0 = comB + (j >> 1) * 4096 + (((2 * (j & 1)) ^ f2) * 32) + pp * 8;
            adB1 = comB + 2 * 4096 + ((((pp >> 1) * 2 + (j >> 1)) ^ f2) * 32) + (j & 1) * 16 + (pp & 1) * 8;
        }
    }
    f32x4 acc[8][5], acch[2];
#pragma unroll
    for (int i = 0; i < 8; ++i)
#pragma unroll
        for (int c = 0; c < 5; ++c) { acc[i][c] = f32x4{0, 0, 0, 0}; asm volatile("" : "+v"(acc[i][c])); }
    acch[0] = acch[1] = f32x4{0, 0, 0, 0};
    asm volatile("" : "+v"(acch[0]), "+v"(acch[1]));
    bf16x8 fa[6], fb[5];
    unsigned ra = adA0, rb0 = adB0, rb1 = adB1, rb2 = adB0 ^ 32;
    unsigned bo = 0, ao = 0;
    auto rdB = [&](auto R) {
        constexpr int r = R;
        if constexpr (TA) fb[r] = lds_b(rb0 + r * 4096);
        else fb[r] = (r == 0) ? lds_b(rb0) : (r == 1) ? lds_b(rb2) : (r == 2) ? lds_b(rb0 + 3 * 4096) : (r == 3) ? lds_b(rb2 + 3 * 4096) : lds_b(rb1);
    };
    auto mm = [&](auto I, auto C, auto SL) {
        constexpr int i = I, c = C, sl = SL;
        if constexpr (TA && c == 2) acc[i][c] = mfma32(fa[sl], fb[c], acc[i][c]);
        else acc[i][c] = mfma32(fb[c], fa[sl], acc[i][c]);
    };
    auto step = [&](auto PAR) {
        constexpr int par = PAR, X = par ? 3 : 0, Y = par ? 0 : 3;
        sfor<15>([&](auto I) {
            constexpr int idx = I, c = idx / 3, i = idx % 3;
            mm(IC(i), IC(c), IC(X + i));
            if constexpr (idx == 0) fa[Y] = lds_a(ra + 4 * 2048);
            if constexpr (idx == 1) fa[Y + 1] = lds_a(ra + 5 * 2048);
            if constexpr (idx == 2 && TA) fa[Y + 2] = lds_a(ra);
            if constexpr (idx == 4) { bo = (bo == (NB - 1) * BBLK) ? 0u : bo + BBLK; if constexpr (par == 1) ao = (ao == (NA - 1) * ABLK) ? 0u : ao + ABLK; asm volatile("" : "+s"(bo), "+s"(ao)); }
            if constexpr (idx == 6) { rb0 = adB0 + bo; asm volatile("" : "+v"(rb0)); }
            if constexpr (idx == 8 && !TA) { rb1 = adB1 + bo; asm volatile("" : "+v"(rb1)); }
            if constexpr (idx == 10 && !TA) { rb2 = rb0 ^ 32; asm volatile("" : "+v"(rb2)); }
            SB();
        });
        sfor<10>([&](auto I) {
            constexpr int idx = I, c = idx / 2, i = idx % 2;
            mm(IC(3 + i), IC(c), IC(Y + i));
            if constexpr (idx < 3) fa[X + idx] = lds_a(ra + (6 + idx) * 2048);
            if constexpr (idx == 4) { ra = (par == 0) ? (ra ^ 64) : adA0 + ao; asm volatile("" : "+v"(ra)); }
            SB();
        });
        if constexpr (TA) {
            acch[0] = mfma32(fb[1], fa[Y + 2], acch[0]);
            acch[1] = mfma32(fa[Y + 2], fb[2], acch[1]);
        }
        SB();
        bar_lgkm();
        SB();
        sfor<5>([&](auto C) {
            constexpr int c = C;
            mm(IC(5), C, IC(X));
            if constexpr (c < 3) fa[Y + c] = lds_a(ra + (1 + c) * 2048);
            SB();
            mm(IC(6), C, IC(X + 1));
            mm(IC(7), C, IC(X + 2));
            SB();
            rdB(C);
            SB();
        });
    };
    __builtin_amdgcn_s_setprio(1);
    SB();
    bar_lgkm();
    SB();
    sfor<5>([&](auto C) {
        constexpr int c = C;
        if constexpr (c < 3) fa[c] = lds_a(ra + (1 + c) * 2048);
        SB();
        rdB(C);
        SB();
    });
#pragma unroll 1
    for (int s = 0; s < 16; s += 2) {
        step(IC(0));
        step(IC(1));
    }
    __builtin_amdgcn_s_setprio(0);
    constexpr int OST = TA ? 144 : 336;
    const unsigned ow = L_A + tok * OST + g * 8;
    auto cvt4 = [&](f32x4 a) { return u32x2{pk2(a[0], a[1]), pk2(a[2], a[3])}; };
    auto hmul = [&](f32x4 ga, f32x4 ua) {
        f32x4 t;
#pragma unroll
        for (int e = 0; e < 4; ++e) t[e] = fmaxf(ga[e], 0.f);
        return cvt4(t * ua);
    };
    if (TA) {
#pragma unroll
        for (int t = 0; t < 8; ++t) LDS(u32x2, ow + 16 * t * OST + j * 32) = hmul(acc[t][3], acc[t][4]);
        bar_lgkm();
        const unsigned oo = L_B + 16384 + tok * 128 + (((2 * j + (g >> 1)) ^ (tok & 7)) * 16) + (g & 1) * 8;
        const bool par = tok & 1;
        const int da = tok - (4 * g + (tok & 1));
        const bool ca = da >= 0, cb = da >= 2;
        const f32x4 z = {0, 0, 0, 0};
        s16x4 kprev = __builtin_bit_cast(s16x4, cvt4(acch[0]));
        u32x2 vprev = cvt4(acch[1]);
#pragma unroll
        for (int hb = 0; hb < 2; ++hb) {
            s16x4 kf[5], qf[4]; u32x2 vf[5];
            kf[0] = kprev; vf[0] = vprev;
#pragma unroll
            for (int i = 0; i < 4; ++i) {
                kf[i + 1] = __builtin_bit_cast(s16x4, cvt4(acc[4 * hb + i][1]));
                qf[i] = __builtin_bit_cast(s16x4, cvt4(acc[4 * hb + i][0]));
            }
            f32x4 sp[4], sc[4];
#pragma unroll
            for (int i = 0; i < 4; ++i) {
                sp[i] = __builtin_amdgcn_mfma_f32_16x16x16bf16_1k(kf[i], qf[i], z, 0, 0, 0);
                sc[i] = __builtin_amdgcn_mfma_f32_16x16x16bf16_1k(kf[i + 1], qf[i], z, 0, 0, 0);
            }
#pragma unroll
            for (int i = 0; i < 4; ++i) vf[i + 1] = cvt4(acc[4 * hb + i][2]);
            kprev = kf[4]; vprev = vf[4];
            unsigned lw[4];
#pragma unroll
            for (int i = 0; i < 4; ++i) {
                asm volatile("" : "+v"(sp[i]), "+v"(sc[i]));
                const float a0 = par ? sc[i][1] : sc[i][0], a1 = par ? sp[i][1] : sp[i][0];
                const float b0 = par ? sc[i][3] : sc[i][2], b1 = par ? sp[i][3] : sp[i][2];
                float la = ca ? a0 : a1, lb = cb ? b0 : b1;
                if (i == 0 && hb == 0) { if (p == 0) { la = ca ? la : -3.0e38f; lb = cb ? lb : -3.0e38f; } }
                lw[i] = pk2(la, lb);
            }
            tr4(lw);
            float e8[8];
#pragma unroll
            for (int i = 0; i < 4; ++i) { e8[2 * i] = bflo(lw[i]); e8[2 * i + 1] = bfhi(lw[i]); }
            const float mx = fmaxf(fmaxf(fmaxf(e8[0], e8[1]), fmaxf(e8[2], e8[3])), fmaxf(fmaxf(e8[4], e8[5]), fmaxf(e8[6], e8[7])));
            float sum = 0;
#pragma unroll
            for (int i = 0; i < 8; ++i) { e8[i] = __builtin_amdgcn_exp2f((e8[i] - mx) * (0.25f * LOG2E)); sum += e8[i]; }
            const float inv = __builtin_amdgcn_rcpf(sum);
#pragma unroll
            for (int i = 0; i < 4; ++i) lw[i] = pk2(e8[2 * i] * inv, e8[2 * i + 1] * inv);
            tr4(lw);
#pragma unroll
            for (int i = 0; i < 4; ++i) {
                const unsigned pw = lw[i];
                const unsigned wa = par ? (pw << 16) : (pw & 0xffffu), wb = par ? (pw & 0xffff0000u) : (pw >> 16);
                const u32x4 pp = {ca ? 0u : wa, cb ? 0u : wb, ca ? wa : 0u, cb ? wb : 0u};
                const u32x4 vv = {vf[i][0], vf[i][1], vf[i + 1][0], vf[i + 1][1]};
                sp[i] = mfma32(__builtin_bit_cast(bf16x8, vv), __builtin_bit_cast(bf16x8, pp), z);
            }
#pragma unroll
            for (int i = 0; i < 4; ++i) LDS(u32x2, oo + ((4 * hb + i) >> 1) * BBLK + (i & 1) * 2048) = cvt4(sp[i]);
        }
    } else {
        const int jj = j0 - 8, odd = jj & 1;
        const int lo_off = (odd ? 64 : 0) + j * 64;
        const int mx_off = (odd ? 0 : 256) + j * 16;
#pragma unroll
        for (int i = 0; i < 8; ++i)
#pragma unroll
            for (int c = 0; c < 2; ++c) LDS(u32x2, ow + 16 * i * OST + lo_off + c * 32) = hmul(acc[i][c], acc[i][c + 2]);
#pragma unroll
        for (int i = 0; i < 8; i += 2) {
            f32x4 ga, ua;
#pragma unroll
            for (int e = 0; e < 4; ++e) {
                unsigned a = __float_as_uint(acc[i][4][e]), b = __float_as_uint(acc[i + 1][4][e]);
                swap32(a, b);
                ga[e] = __uint_as_float(a); ua[e] = __uint_as_float(b);
            }
            LDS(u32x2, L_A + (16 * (i + (g >> 1)) + tok) * OST + mx_off + (g & 1) * 8) = hmul(ga, ua);
        }
        store_h(a2, false, p, j0);
    }
}

constexpr int a2slot(int st) { return (st & 3) == 3 ? L_B : L_A + (st & 3) * ABLK; }
constexpr int cnt_iv(int t) { return t < 12 ? ((t & 1) ? 7 : 8) : (t == 12 ? 9 : 4); }
constexpr int wait_iv(int t) {
    return t < 2 ? 15 : (t == 14 ? 4 : 0) + cnt_iv(t - 1) + cnt_iv(t);
}

__global__ __launch_bounds__(512, 2) void kfused(const u16* x, const u16* wqkv, const u16* wo, const u16* wg, const u16* wu, const u16* wd,
                                                 u16* a2, u16* out, unsigned* flags) {
    const int w = __builtin_amdgcn_readfirstlane(threadIdx.x >> 6);
    const int b = blockIdx.x, xcd = b & 7, loc = b >> 3;
    if (w == 7) {
        unsigned long long pc;
        asm volatile("s_getpc_b64 %0" : "=s"(pc));
        const int l = threadIdx.x & 63;
        int ln = (l < 32) ? l : 32 + loc * 7 + ((l - 32) % 7);
        ln = ln < CODE_LINES ? ln : 0;
        asm volatile("s_mov_b32 m0, %0\n\ts_nop 0" :: "s"(L_DUMP));
        touch_s((const char*)(pc & ~127ull), (unsigned)(ln * 128));
    }
    const int ci = loc >> 3;
    const int p = (xcd & 1) * 8 + (loc & 7), qd = xcd >> 1;
    const int j0 = (ci < 2) ? 2 * qd + ci : 6 + 2 * qd + ci;
    const int rt = (xcd & 1) * 16 + (xcd >> 1) * 4 + (loc & 3), ct = loc >> 2;
    const int R0 = rt * 64, C0 = ct * 64;
    const bool TA = ci < 2;
    u16* const xn = a2 + (5 << 20);
    unsigned* const inbox = flags + 16384;
    ld_row(L_A + w * 1024, x, p, 16 + 8 * j0 + w);
    if (w == 0) ld_row(L_A + 8192, x, p, j0);
    if (w == 3) touch_v_sc1(inbox + (p * 16 + j0) * 32 + (threadIdx.x & 31), L_FP);
    if (threadIdx.x == 0) LDS(volatile u32x4, L_CNT) = u32x4{0u, 0u, 0u, 0u};
    bar_lgkm();

    const int h = w & 3;
    const int l = threadIdx.x & 63, kq = l >> 3;
    const int lchB = (l & 7) ^ ((((kq >> 1) & 1) | ((h & 1) << 1)) << 1);
    const char *sB0, *sB1, *sB3, *sB4, *gM; unsigned vB0, vB1, st0;
    {
        const int k = 8 * h + kq;
        const int jj = j0 - 8, f0 = 512 + 160 * jj, odd = jj & 1;
        st0 = TA ? 3072u : (unsigned)(F_ * 2);
        vB1 = (unsigned)(k * (F_ * 2) + lchB * 16);
        vB0 = (unsigned)(k * st0 + lchB * 16);
        sB0 = TA ? (const char*)(wqkv + 64 * j0) : (const char*)(wg + f0 + (odd ? 32 : 0));
        sB1 = sB0 + (TA ? 1024 : 128);
        sB3 = TA ? (const char*)(wg + 64 * j0) : (const char*)(wu + f0 + (odd ? 32 : 0));
        sB4 = TA ? (const char*)(wu + 64 * j0) : sB3 + 128;
        const char* m0_ = (const char*)(wg + f0 + (odd ? 0 : 128)), *m1_ = (const char*)(wu + f0 + (odd ? 0 : 128)) - 64;
        gM = (TA ? sB0 + 2048 : (lchB < 4 ? m0_ : m1_)) + vB0;
    }
    const unsigned dB = L_B + h * 1024;
    auto issB = [&](auto SLOT) {
        constexpr int so = SLOT * BBLK;
        setm0<so>(dB); nop1(); dma_s<0>(sB0, vB0);
        setm0<so + 4096>(dB); nop1(); dma_s<0>(sB1, vB0);
        setm0<so + 8192>(dB); nop1(); dma_v(gM);
        setm0<so + 12288>(dB); nop1(); dma_s<0>(sB3, vB1);
        setm0<so + 16384>(dB); nop1(); dma_s<0>(sB4, vB1);
        vB1 += 32 * F_ * 2; vB0 += 32 * st0; gM += 32 * st0;
    };
    if (w >= 4) issB(IC(0));
    ln_exchange(xn, inbox, w, p, j0);

    if (w >= 4) {
        issB(IC(1));
        wait_vm<2>();
        issB(IC(2));
        unsigned* const myP = flags + 32768 + (p * 16 + (l & 15)) * 32 + j0;
        const unsigned* const myE = flags + 512 + (p * 16 + j0) * 32 + (l & 15);
        const unsigned* const pollP = flags + 32768 + ((rt >> 1) * 16 + (rt & 1) * 8 + ct) * 32 + (l & 15);
        unsigned* const echoE = flags + 512 + ((rt >> 1) * 16 + (l & 15)) * 32 + (rt & 1) * 8 + ct;
        unsigned myv = 0, last = 2;
        const char* xs = (const char*)(xn + (size_t)p * 144 * 512);
        unsigned vA[4], vH;
        {
            const int rr = 8 * h + kq;
            vA[0] = (unsigned)((16 + rr) * 1024 + (((l & 7) ^ ((rr >> 1) & 7)) * 16));
            vA[1] = vA[0] + 32768; vA[2] = vA[0] + 65536; vA[3] = vA[0] + 98304;
            const int hr = 4 * h + (kq & 3);
            vH = (unsigned)(hr * 1024 + (((l & 7) ^ ((hr >> 1) & 7)) * 16));
        }
        const unsigned dA = L_A + 2048 + h * 1024, dH = L_A + h * 512;
        auto issA = [&](auto BLK, auto HALF) {
            constexpr int S = BLK, hf = HALF, so = (S % NA) * ABLK - S * 128;
            setm0<so + hf * 8192>(dA); nop1(); dma_s_sc1<S * 128>(xs, vA[2 * hf]);
            setm0<so + hf * 8192 + 4096>(dA); nop1(); dma_s_sc1<S * 128>(xs, vA[2 * hf + 1]);
            if constexpr (hf == 0) { setm0<so>(dH); dma_s32_sc1<S * 128>(xs, vH); }
        };
        const unsigned vW = (unsigned)((8 * h + kq) * 1024 + lchB * 16);
        const char* const wdc = (const char*)(wd + C0);
        const char* const woc = (const char*)(wo + C0);
        auto issW = [&](auto ST) {
            constexpr int st = ST, so = (1 + (st & 3)) * BBLK;
            const char* base = (st < 14) ? wdc + st * 131072 : woc + (st - 14) * 131072;
            sfor<4>([&](auto C) { constexpr int c = C; setm0<so + c * 4096>(dB); nop1(); dma_s<0>(base + c * 32768, vW); });
        };
        auto warm1 = [&](auto T) {
            constexpr int t = T;
            if (h == (t & 3)) {
                const int row = 224 + 36 * (loc & 7) + (l < 36 ? l : 35);
                const char* a;
                if (TA) a = (t < 3) ? (const char*)(wqkv + 512 * t + 64 * j0) + row * 3072 : (const char*)((t & 1 ? wg : wu) + 64 * j0) + row * (F_ * 2);
                else a = (const char*)((t & 1 ? wg : wu) + ((512 + 160 * (j0 - 8)) & ~63) + 64 * (t >> 1)) + row * (F_ * 2);
                touch_v(a, L_DUMP);
            }
        };
        auto warm2 = [&](auto R) {
            constexpr int r = R;
            if (h == ((r + 1) & 3)) {
                const int vr = (r * 32 + loc) * 8;
                const char* a = (r < 7) ? (const char*)wd + vr * 1024 : (const char*)wo + (vr - 1792) * 1024;
                touch_v(a + l * 128, L_DUMP);
            }
        };
        bar();
        issA(IC(0), IC(0)); issA(IC(0), IC(1));
        issA(IC(1), IC(0)); issA(IC(1), IC(1));
        wait_bar<5>();
        if (h == 0) touch_v_sc1(myE, L_FE);
        sfor<16>([&](auto T) {
            constexpr int t = T;
            if constexpr (t < 12) issA(IC(t / 2 + 2), IC(t & 1));
            if constexpr (t < 13) issB(IC((t + 3) % NB));
            if constexpr (t >= 12) issW(IC(t - 12));
            if constexpr (t >= W2_AT && t < W2_AT + 9) warm2(IC(t - W2_AT));
            if constexpr (t == 2) {
                if (h == 0) {
                    const unsigned ev = LDS(const volatile unsigned, L_FE + l * 4);
                    unsigned v = 0;
                    while (__ballot(ev == v) != 0ull) v += 4;
                    myv = v;
                    st_sc1(myP, v, 0xffffull);
                }
            }
            if constexpr (t == 6) { if (h == 0) touch_v_sc1(pollP, L_FP); }
            if constexpr (t == 10) {
                if (h == 0) {
                    const unsigned pv = LDS(const volatile unsigned, L_FP + l * 4);
                    st_sc1(echoE, pv, __ballot(!(pv & 3)) & 0xffffull);
                    last = (pv & 3) ? last : pv;
                }
            }
            if constexpr (t == 14) { if (h == 0) touch_v_sc1(myE, L_FE); }
            if constexpr (t < 15) wait_bar<wait_iv(t)>(); else bar();
        });
        unsigned vA2[2];
        { const int row = 8 * h + kq; vA2[0] = (unsigned)(row * (A2LD * 2) + (((l & 7) ^ ((row >> 1) & 7)) * 16)); vA2[1] = vA2[0] + 32 * A2LD * 2; }
        const char* const sa = (const char*)(a2 + (size_t)R0 * A2LD);
        const unsigned dA2 = h * 1024;
        const char* tA;
        { int id = h * 40 + (l < 40 ? l : 0); id += ct * 160; tA = sa + (size_t)(id & 63) * (A2LD * 2) + 2048 + (id >> 6) * 128; }
        store_h(a2, TA, p, j0);
        unsigned ev = 0, state = 0, shown = 0;
        auto serve = [&](unsigned pv, unsigned long long mask, unsigned ign, bool flush) {
#pragma unroll 1
            for (unsigned spin = 0; spin < SPIN_CAP; ++spin) {
                if (state == 0 && __builtin_amdgcn_readfirstlane(LDS(const volatile unsigned, L_CNT + 4)) == (TA ? 4u : 8u)) state = TA ? 1u : 3u;
                if (state != shown && __ballot(ev != myv) == 0ull) { st_sc1(myP, myv + state, 0xffffull); shown = state; }
                st_sc1(echoE, pv, __ballot(!(pv & 3) && pv != last) & 0xffffull);
                last = (pv & 3) ? last : pv;
                if ((__ballot(((pv - last) | ign) != 3u || (last & 3)) & mask) == 0ull && !(flush && shown != 3u)) break;
                ld2_sc1(pv, pollP, ev, myE);
            }
        };
        if (h == 0) {
            ev = LDS(const volatile unsigned, L_FE + l * 4);
            serve(last, 0xffull, 2u, false);
            if (l == 0) LDS(volatile unsigned, L_CNT + 8) = 1u;
        }
        lds_wait(L_CNT + 12, TA ? 4u : 8u);
        lds_wait(L_CNT + 8, 1u);
        auto issA2 = [&](auto ST) {
            constexpr int st = ST, ko = (st < 14) ? 1024 + st * 256 : (st - 14) * 256, so = a2slot(st);
            const char* sk = sa + ko;
            setm0<so>(dA2); nop1(); dma_s_sc1<0>(sk, vA2[0]);
            setm0<so + 4096>(dA2); nop1(); dma_s_sc1<0>(sk, vA2[1]);
            setm0<so + 8192 - 128>(dA2); nop1(); dma_s_sc1<128>(sk, vA2[0]);
            setm0<so + 12288 - 128>(dA2); nop1(); dma_s_sc1<128>(sk, vA2[1]);
        };
        issA2(IC(0)); issA2(IC(1));
        if (h == 0) serve(last, 0ull, 0u, false);
        wait_vm<4>();
        bar();
        if (h == 0) { serve(last, 0ull, 0u, false); touch_v_sc1(pollP, L_FP); }
        if (TA) {
            const __amdgpu_buffer_rsrc_t a2r = __builtin_amdgcn_make_buffer_rsrc(a2, 0, S_ * A2LD * 2, 0x00020000);
            const int t4 = threadIdx.x & 255, r_ = t4 >> 3;
            const unsigned rd = L_B + 16384 + r_ * 128 + (((t4 & 7) ^ (r_ & 7)) * 16);
            const unsigned go = ((p * 128 + r_) * A2LD + 64 * j0 + (t4 & 7) * 8) * 2;
            u32x4 v[4];
#pragma unroll
            for (int it = 0; it < 4; ++it) v[it] = LDS(const u32x4, rd + it * BBLK);
#pragma unroll
            for (int it = 0; it < 4; ++it) __builtin_amdgcn_raw_buffer_store_b128(v[it], a2r, go, it * 32 * A2LD * 2, 16);
        }
        issA2(IC(2)); issA2(IC(3));
        if (TA) wait_vm<12>(); else wait_vm<8>();
        bar();
        sfor<16>([&](auto ST) {
            constexpr int st = ST;
            if constexpr (st == 0) {
                issW(IC(4));
                if (h == 0) {
                    serve(LDS(const volatile unsigned, L_FP + l * 4), 0xff00ull, 2u, false);
                    if (l == 0) LDS(volatile unsigned, L_CNT + 8) = 2u;
                }
                lds_wait(L_CNT + 8, 2u);
                issA2(IC(4));
                touch_v_sc1(tA, L_DUMP);
            } else if constexpr (st < 14) { issA2(IC(st + 4)); issW(IC(st + 4)); }
            if constexpr (st == 1) { if (h == 0) { state = 3u; serve(last, 0ull, 0u, false); } }
            if constexpr (st == 6) { if (h == 0) touch_v_sc1(pollP, L_FP); }
            if constexpr (st == 9) { if (h == 0) serve(LDS(const volatile unsigned, L_FP + l * 4), 0xffull, 0u, true); }
            wait_bar<(st == 0) ? 12 : (st < 14) ? 16 : (st == 14) ? 8 : 0>();
        });
        return;
    }
    if (w < 3) {
        const int row = 96 + 52 * (loc & 7) + (l < 52 ? l : 51);
#pragma unroll
        for (int i = 0; i < 2; ++i) {
            const int t = 2 * w + i;
            const char* a;
            if (TA) a = (t < 3) ? (const char*)(wqkv + 512 * t + 64 * j0) + row * 3072 : (const char*)((t & 1 ? wg : wu) + 64 * j0) + row * (F_ * 2);
            else a = (const char*)((t & 1 ? wg : wu) + ((512 + 160 * (j0 - 8)) & ~63) + 64 * (t >> 1)) + row * (F_ * 2);
            touch_v(a, L_DUMP);
            if (i == 0) wait_vm<0>();
        }
        bar();
    }
    if (TA) p1_mfma<true>(a2, p, j0); else p1_mfma<false>(a2, p, j0);
    {
        int l2 = threadIdx.x & 63;
        asm volatile("" : "+v"(l2));
        const int g = l2 >> 4, tok = l2 & 15;
        const int wm = w >> 1, wn = w & 1;
        const unsigned a2A = L_A + (32 * wm + tok) * 128 + ((g ^ (tok >> 1)) * 16);
        const unsigned a2B = L_B + (8 * g + (tok >> 2)) * 128 + (((2 * wn) ^ (((tok >> 3) & 1) | ((g & 1) << 1))) * 32) + (l2 & 3) * 8;
        const size_t orow = (size_t)(R0 + 32 * wm + tok) * D_ + C0 + 32 * wn + 4 * g;
        u32x2 xv[2][2];
#pragma unroll
        for (int i = 0; i < 2; ++i)
#pragma unroll
            for (int c = 0; c < 2; ++c) xv[i][c] = *(const u32x2*)(x + orow + i * 16 * D_ + 16 * c);
        f32x4 c2[2][2][2];
#pragma unroll
        for (int a = 0; a < 2; ++a)
#pragma unroll
            for (int i = 0; i < 2; ++i)
#pragma unroll
                for (int c = 0; c < 2; ++c) c2[a][i][c] = f32x4{0, 0, 0, 0};
        bf16x8 ga[2][4][2], gb[2][4][2];
        const unsigned pa0 = a2A, pa1 = a2A ^ 64, pa2 = a2A - L_A, pa3 = pa2 ^ 64;
        const unsigned pb0 = a2B + BBLK, pb1 = pb0 ^ 32, pb2 = pb0 + 2 * BBLK, pb3 = pb2 ^ 32;
        auto rd = [&](auto SET, auto I, auto Q) {
            constexpr int set = SET, i = I, ks = i >> 1, q = Q;
            const unsigned ab = (q == 3) ? ((ks & 1) ? pa3 : pa2) : ((ks & 1) ? pa1 : pa0);
            const unsigned bb = (q >= 2) ? ((i & 1) ? pb3 : pb2) : ((i & 1) ? pb1 : pb0);
            ga[set][ks][i & 1] = lds_a(ab + (q == 3 ? 0 : q * ABLK) + (ks >> 1) * 8192 + (i & 1) * 2048);
            gb[set][ks][i & 1] = lds_b(bb + (q & 1) * BBLK + ks * 4096);
        };
        auto stage = [&](auto Q, auto ACC, auto LAST) {
            constexpr int q = Q, set = q & 1, ac = ACC; constexpr bool lastst = LAST;
            if constexpr (!lastst) bar_lgkm();
            SB();
            sfor<16>([&](auto I) {
                constexpr int idx = I, ks = idx >> 2, i = (idx >> 1) & 1, c = idx & 1;
                c2[ac][i][c] = mfma32(gb[set][ks][c], ga[set][ks][i], c2[ac][i][c]);
                if constexpr (!lastst && idx < 8) rd(IC(set ^ 1), I, IC((q + 1) & 3));
                SB();
            });
        };
        typedef std::integral_constant<bool, true> T_; typedef std::integral_constant<bool, false> F_t;
        asm volatile("s_waitcnt vmcnt(0)" : "+v"(xv[0][0]), "+v"(xv[0][1]), "+v"(xv[1][0]), "+v"(xv[1][1]) :: "memory");
        bar_lgkm();
        sfor<8>([&](auto I) { rd(IC(0), I, IC(0)); });
#pragma unroll 1
        for (int it = 0; it < 3; ++it) {
            stage(IC(0), IC(1), F_t{}); stage(IC(1), IC(1), F_t{}); stage(IC(2), IC(1), F_t{}); stage(IC(3), IC(1), F_t{});
        }
        stage(IC(0), IC(1), F_t{}); stage(IC(1), IC(1), F_t{});
        stage(IC(2), IC(0), F_t{}); stage(IC(3), IC(0), F_t{});
        stage(IC(0), IC(0), F_t{}); stage(IC(1), IC(0), T_{});
#pragma unroll
        for (int i = 0; i < 2; ++i)
#pragma unroll
            for (int c = 0; c < 2; ++c) {
                float r[4];
#pragma unroll
                for (int e = 0; e < 4; ++e) {
                    const float d = rbf(c2[1][i][c][e]);
                    const float sg = rbf(d * __builtin_amdgcn_rcpf(1.f + __builtin_amdgcn_exp2f(-d * LOG2E)));
                    const float t = rbf(rbf(c2[0][i][c][e]) * sg);
                    const float xx = (e & 1) ? bfhi(xv[i][c][e >> 1]) : bflo(xv[i][c][e >> 1]);
                    r[e] = xx + t;
                }
                *(u32x2*)(out + orow + i * 16 * D_ + 16 * c) = u32x2{pk2(r[0], r[1]), pk2(r[2], r[3])};
            }
    }
}

extern "C" void kernel_launch(void* const* d_in, const int* in_sizes, int n_in,
                              void* d_out, int out_size, void* d_ws, size_t ws_size,
                              hipStream_t stream) {
    (void)hipFuncSetAttribute((const void*)kfused, hipFuncAttributeMaxDynamicSharedMemorySize, K_LDS);
    kfused<<<256, 512, K_LDS, stream>>>((const u16*)d_in[0], (const u16*)d_in[1], (const u16*)d_in[2], (const u16*)d_in[4], (const u16*)d_in[5],
                                        (const u16*)d_in[6], (u16*)d_ws, (u16*)d_out, (unsigned*)((char*)d_ws + (16 << 20)));
}
```

```cpp
#include <hip/hip_runtime.h>
#include <stdint.h>
#include <utility>

typedef unsigned short u16;
typedef __attribute__((ext_vector_type(8))) __bf16 bf16x8;
typedef __attribute__((ext_vector_type(2))) __bf16 bf16x2;
typedef __attribute__((ext_vector_type(4))) short s16x4;
typedef __attribute__((ext_vector_type(8))) short s16x8;
typedef __attribute__((ext_vector_type(4))) float f32x4;
typedef __attribute__((ext_vector_type(2))) float f32x2;
typedef __attribute__((ext_vector_type(4))) unsigned u32x4;
typedef __attribute__((ext_vector_type(2))) unsigned u32x2;
#define LAS __attribute__((address_space(3)))
#define SB() __builtin_amdgcn_sched_barrier(0)
#define IC(n) std::integral_constant<int, (n)>{}
#define LDS(T, a) (*(LAS T*)(uintptr_t)(a))

constexpr int S_ = 2048, D_ = 512, F_ = 1792;
constexpr int A2LD = 2304;
constexpr float LOG2E = 1.4426950408889634f;

template <int... I, class Fn> __device__ __forceinline__ void sfor_impl(std::integer_sequence<int, I...>, Fn&& f) { (f(std::integral_constant<int, I>{}), ...); }
template <int N, class Fn> __device__ __forceinline__ void sfor(Fn&& f) { sfor_impl(std::make_integer_sequence<int, N>{}, f); }

__device__ __forceinline__ float rbf(float f) { return (float)(__bf16)f; }
__device__ __forceinline__ unsigned pk2(float lo, float hi) {
    bf16x2 v; v[0] = (__bf16)lo; v[1] = (__bf16)hi; return __builtin_bit_cast(unsigned, v);
}
__device__ __forceinline__ float bflo(unsigned u) { return __uint_as_float(u << 16); }
__device__ __forceinline__ float bfhi(unsigned u) { return __uint_as_float(u & 0xffff0000u); }

template <int OFF> __device__ __forceinline__ void setm0(unsigned base) {
    asm volatile("s_add_u32 m0, %0, %1" :: "s"(base), "n"(OFF) : "scc");
}
__device__ __forceinline__ void dma_v(const void* g) { asm volatile("global_load_lds_dwordx4 %0, off" :: "v"(g) : "memory"); }
template <int IO> __device__ __forceinline__ void dma_s(const void* sb, unsigned vo) {
    asm volatile("global_load_lds_dwordx4 %0, %1 offset:%2" :: "v"(vo), "s"(sb), "n"(IO) : "memory");
}
template <int IO> __device__ __forceinline__ void dma_s_sc1(const void* sb, unsigned vo) {
    asm volatile("global_load_lds_dwordx4 %0, %1 offset:%2 sc1" :: "v"(vo), "s"(sb), "n"(IO) : "memory");
}
template <int IO> __device__ __forceinline__ void dma_s32_sc1(const void* sb, unsigned vo) {
    asm volatile("s_mov_b64 exec, %3\n\tglobal_load_lds_dwordx4 %0, %1 offset:%2 sc1\n\ts_mov_b64 exec, -1" :: "v"(vo), "s"(sb), "n"(IO), "s"(0xffffffffull) : "memory");
}
__device__ __forceinline__ void touch_s(const void* sb, unsigned vo) {
    asm volatile("global_load_lds_dword %0, %1" :: "v"(vo), "s"(sb) : "memory");
}
__device__ __forceinline__ unsigned ld_sc1(const unsigned* p) {
    unsigned a; asm volatile("global_load_dword %0, %1, off sc1\n\ts_waitcnt vmcnt(0)" : "=&v"(a) : "v"(p) : "memory"); return a;
}
__device__ __forceinline__ void nop1() { asm volatile("s_nop 0"); }
__device__ __forceinline__ void touch_v(const void* gaddr, unsigned ldsaddr) {
    asm volatile("s_mov_b32 m0, %1\n\ts_nop 0\n\tglobal_load_lds_dword %0, off" :: "v"(gaddr), "s"(ldsaddr) : "memory");
}
__device__ __forceinline__ void touch_v_sc1(const void* gaddr, unsigned ldsaddr) {
    asm volatile("s_mov_b32 m0, %1\n\ts_nop 0\n\tglobal_load_lds_dword %0, off sc1" :: "v"(gaddr), "s"(ldsaddr) : "memory");
}
template <int N> __device__ __forceinline__ void wait_vm() { asm volatile("s_waitcnt vmcnt(%0)" :: "n"(N) : "memory"); }
template <int N> __device__ __forceinline__ void wait_bar() {
    __builtin_amdgcn_s_waitcnt(0xC07F);
    asm volatile("s_waitcnt vmcnt(%0)\n\ts_barrier" :: "n"(N) : "memory");
}
__device__ __forceinline__ void bar_lgkm() { __builtin_amdgcn_s_waitcnt(0xC07F); asm volatile("s_barrier" ::: "memory"); }
__device__ __forceinline__ void bar() { asm volatile("s_barrier" ::: "memory"); }
__device__ __forceinline__ void ld2_sc1(unsigned& a, const unsigned* pa, unsigned& b, const unsigned* pb) {
    asm volatile("global_load_dword %0, %2, off sc1\n\tglobal_load_dword %1, %3, off sc1\n\ts_waitcnt vmcnt(0)"
                 : "=&v"(a), "=&v"(b) : "v"(pa), "v"(pb) : "memory");
}
__device__ __forceinline__ void st_sc1(unsigned* p, unsigned v, unsigned long long mask) {
    asm volatile("s_mov_b64 exec, %2\n\ts_cbranch_execz 1f\n\tglobal_store_dword %0, %1, off sc1\n1:\n\ts_mov_b64 exec, -1"
                 :: "v"(p), "v"(v), "s"(mask) : "memory");
}

__device__ __forceinline__ bf16x8 lds_a(unsigned addr) { return LDS(const bf16x8, addr); }
__device__ __forceinline__ bf16x8 lds_b(unsigned addr) {
    s16x4 lo = __builtin_amdgcn_ds_read_tr16_b64_v4i16((LAS s16x4*)(uintptr_t)addr);
    s16x4 hi = __builtin_amdgcn_ds_read_tr16_b64_v4i16((LAS s16x4*)(uintptr_t)(addr + 512));
    s16x8 r = __builtin_shufflevector(lo, hi, 0, 1, 2, 3, 4, 5, 6, 7);
    return __builtin_bit_cast(bf16x8, r);
}
__device__ __forceinline__ f32x4 mfma32(bf16x8 a, bf16x8 b, f32x4 c) { return __builtin_amdgcn_mfma_f32_16x16x32_bf16(a, b, c, 0, 0, 0); }
__device__ __forceinline__ void swap16(unsigned& a, unsigned& b) { auto r = __builtin_amdgcn_permlane16_swap(a, b, false, false); a = r[0]; b = r[1]; }
__device__ __forceinline__ void swap32(unsigned& a, unsigned& b) { auto r = __builtin_amdgcn_permlane32_swap(a, b, false, false); a = r[0]; b = r[1]; }
__device__ __forceinline__ void tr4(unsigned (&v)[4]) { swap16(v[0], v[1]); swap16(v[2], v[3]); swap32(v[0], v[2]); swap32(v[1], v[3]); }
__device__ __forceinline__ float xadd16(float v) { unsigned a = __float_as_uint(v), b = a; swap16(a, b); return __uint_as_float(a) + __uint_as_float(b); }
template <int CTRL> __device__ __forceinline__ float dadd(float v) {
    return v + __int_as_float(__builtin_amdgcn_update_dpp(0, __float_as_int(v), CTRL, 0xf, 0xf, true));
}
__device__ __forceinline__ float xadd32(float v) { unsigned a = __float_as_uint(v), b = a; swap32(a, b); return __uint_as_float(a) + __uint_as_float(b); }
__device__ __forceinline__ float red8(float v) { return dadd<0x141>(dadd<0x4E>(dadd<0xB1>(v))); }
__device__ __forceinline__ float red64(float v) { return xadd32(xadd16(dadd<0x140>(red8(v)))); }

constexpr int NB = 5, NA = 3;
constexpr int BBLK = 20480, ABLK = 18432;
constexpr int L_B = 0, L_A = NB * BBLK, L_ST = L_A + NA * ABLK;
constexpr int L_CNT = L_ST;
constexpr int L_DUMP = L_ST + 64;
constexpr int L_FE = L_DUMP + 256, L_FP = L_FE + 256;
constexpr int K_LDS = L_FP + 256;
constexpr unsigned SPIN_CAP = 1u << 16;
constexpr int W2_AT = 4;
#define CODE_LINES 228

__device__ __forceinline__ void ld_row(unsigned dst, const u16* x, int p, int ir) {
    int gr = p * 128 - 16 + ir; gr = gr < 0 ? 0 : gr;
    setm0<0>(dst); nop1(); dma_s<0>(x + (size_t)gr * 512, (threadIdx.x & 63) * 16);
}
__device__ __forceinline__ void ln_row(u32x4 v, u16* xn, int p, int ir, bool real) {
    float f[8], s = 0;
#pragma unroll
    for (int e = 0; e < 4; ++e) { const unsigned u = v[e]; f[2 * e] = bflo(u); f[2 * e + 1] = bfhi(u); s += f[2 * e] + f[2 * e + 1]; }
    const float sh = __int_as_float(__builtin_amdgcn_readfirstlane(__float_as_int(f[0])));
    float q = 0;
#pragma unroll
    for (int e = 0; e < 8; ++e) { const float d = f[e] - sh; q = fmaf(d, d, q); }
    const float mean = red64(s) * (1.f / 512.f);
    const float dm = mean - sh;
    const float var = fmaxf(red64(q) * (1.f / 512.f) - dm * dm, 0.f);
    const float sd = rbf(__builtin_amdgcn_sqrtf(rbf(rbf(var) + 1e-5f)));
    const float mb = rbf(mean), rc = __builtin_amdgcn_rcpf(sd);
    float o[8];
#pragma unroll
    for (int e = 0; e < 8; ++e) {
        const float a = rbf(f[e] - mb);
        const float q0 = a * rc;
        o[e] = fmaf(fmaf(-q0, sd, a), rc, q0);
    }
    const u32x4 ov = {pk2(o[0], o[1]), pk2(o[2], o[3]), pk2(o[4], o[5]), pk2(o[6], o[7])};
    const __amdgpu_buffer_rsrc_t xr = __builtin_amdgcn_make_buffer_rsrc(xn, 0, 16 * 144 * 1024, 0x00020000);
    if (real) __builtin_amdgcn_raw_buffer_store_b128(ov, xr, (unsigned)((p * 144 + ir) * 1024 + (threadIdx.x & 63) * 16), 0, 16);
    else asm volatile("" :: "v"(ov));
}
__device__ __forceinline__ void ln_done() {
    wait_vm<0>();
    if ((threadIdx.x & 63) == 0) __hip_atomic_fetch_add((LAS unsigned*)(uintptr_t)L_CNT, 1u, __ATOMIC_RELAXED, __HIP_MEMORY_SCOPE_WORKGROUP);
}

__device__ __forceinline__ void ln_exchange(u16* xn, unsigned* inbox, int w, int p, int j0) {
    const int l = threadIdx.x & 63;
    const unsigned* in = inbox + (p * 16 + j0) * 32 + (l & 31);
    unsigned* outp = inbox + (p * 16 + (l & 15)) * 32 + ((l & 16) ? j0 : 16 + j0);
    unsigned want = 0;
    const int npass = (w == 0) ? 3 : 2;
#pragma unroll 1
    for (int pass = 1; pass < npass; ++pass) {
        if (pass == 1) {
            if (w < 4) wait_vm<0>(); else wait_vm<5>();
            if (w == 3) {
                want = LDS(const volatile unsigned, L_FP + l * 4) + 1u;
                st_sc1(outp, want, 0xffff0000ull);
            }
        }
        const u32x4 v = LDS(const volatile u32x4, L_A + (pass == 2 ? 8 : w) * 1024 + l * 16);
        ln_row(v, xn, p, pass == 2 ? j0 : 16 + 8 * j0 + w, pass != 0);
    }
    ln_done();
    if (w != 3) return;
    unsigned u = ld_sc1(in);
    unsigned spin = 0;
#pragma unroll 1
    while (__builtin_amdgcn_readfirstlane(LDS(const volatile unsigned, L_CNT)) != 8u && ++spin < (SPIN_CAP << 4)) {}
    st_sc1(outp, u, 0xffffull);
    bool go = false;
#pragma unroll 1
    for (spin = 0; spin < SPIN_CAP; ++spin) {
        const unsigned echoed = u;
        u = ld_sc1(in);
        st_sc1(outp, u, __ballot(u != echoed) & 0xffffull);
        if (go) break;
        if ((__ballot(u != want) & 0xffff0000ull) == 0ull) { go = true; bar(); }
    }
    if (!go) bar();
}
__device__ __forceinline__ void store_h(u16* a2, bool TA, int p, int j0) {
    int tid = threadIdx.x;
    asm volatile("" : "+v"(tid));
    const __amdgpu_buffer_rsrc_t a2r = __builtin_amdgcn_make_buffer_rsrc(a2, 0, S_ * A2LD * 2, 0x00020000);
    if (TA) {
        const int t4 = tid & 255;
        const unsigned rd = L_A + (t4 >> 3) * 144 + (t4 & 7) * 16;
        const unsigned go = ((p * 128 + (t4 >> 3)) * A2LD + 512 + 64 * j0 + (t4 & 7) * 8) * 2;
        bar_lgkm();
        u32x4 v[4];
#pragma unroll
        for (int it = 0; it < 4; ++it) v[it] = LDS(const u32x4, rd + it * 32 * 144);
#pragma unroll
        for (int it = 0; it < 4; ++it) __builtin_amdgcn_raw_buffer_store_b128(v[it], a2r, go, it * 32 * A2LD * 2, 16);
    } else {
        unsigned rd[5], go[5];
#pragma unroll
        for (int it = 0; it < 5; ++it) {
            const int id = it * 512 + tid, r_ = id / 20, c_ = id - r_ * 20;
            rd[it] = L_A + r_ * 336 + c_ * 16;
            go[it] = ((p * 128 + r_) * A2LD + 1024 + 160 * (j0 - 8) + c_ * 8) * 2;
        }
        bar_lgkm();
        u32x4 v[5];
#pragma unroll
        for (int it = 0; it < 5; ++it) v[it] = LDS(const u32x4, rd[it]);
#pragma unroll
        for (int it = 0; it < 5; ++it) __builtin_amdgcn_raw_buffer_store_b128(v[it], a2r, go[it], 0, 16);
    }
    if ((tid & 63) == 0) __hip_atomic_fetch_add((LAS unsigned*)(uintptr_t)(L_CNT + 12), 1u, __ATOMIC_RELAXED, __HIP_MEMORY_SCOPE_WORKGROUP);
    wait_vm<0>();
    if ((tid & 63) == 0) __hip_atomic_fetch_add((LAS unsigned*)(uintptr_t)(L_CNT + 4), 1u, __ATOMIC_RELAXED, __HIP_MEMORY_SCOPE_WORKGROUP);
}
__device__ __forceinline__ void lds_wait(int addr, unsigned val) {
    unsigned spin = 0;
#pragma unroll 1
    while (__builtin_amdgcn_readfirstlane(LDS(const volatile unsigned, addr)) != val && ++spin < (SPIN_CAP << 6)) {}
}

template <bool TA>
__device__ __forceinline__ void p1_mfma(u16* a2, int p, int j0) {
    int l = threadIdx.x & 63;
    asm volatile("" : "+v"(l));
    const int j = __builtin_amdgcn_readfirstlane(threadIdx.x >> 6);
    const int g = l >> 4, tok = l & 15;
    const unsigned adA0 = L_A + tok * 128 + ((g ^ (tok >> 1)) * 16);
    const int f2 = ((tok >> 3) & 1) | ((g & 1) << 1);
    const unsigned comB = L_B + (8 * g + (tok >> 2)) * 128;
    unsigned adB0, adB1 = 0;
    {
        const int pp = l & 3;
        if (TA) adB0 = comB + ((j ^ f2) * 32) + pp * 8;
        else {
            adB0 = comB + (j >> 1) * 4096 + (((2 * (j & 1)) ^ f2) * 32) + pp * 8;
            adB1 = comB + 2 * 4096 + ((((pp >> 1) * 2 + (j >> 1)) ^ f2) * 32) + (j & 1) * 16 + (pp & 1) * 8;
        }
    }
    f32x4 acc[8][5], acch[2];
#pragma unroll
    for (int i = 0; i < 8; ++i)
#pragma unroll
        for (int c = 0; c < 5; ++c) { acc[i][c] = f32x4{0, 0, 0, 0}; asm volatile("" : "+v"(acc[i][c])); }
    acch[0] = acch[1] = f32x4{0, 0, 0, 0};
    asm volatile("" : "+v"(acch[0]), "+v"(acch[1]));
    bf16x8 fa[6], fb[5];
    unsigned ra = adA0, rb0 = adB0, rb1 = adB1, rb2 = adB0 ^ 32;
    unsigned bo = 0, ao = 0;
    auto rdB = [&](auto R) {
        constexpr int r = R;
        if constexpr (TA) fb[r] = lds_b(rb0 + r * 4096);
        else fb[r] = (r == 0) ? lds_b(rb0) : (r == 1) ? lds_b(rb2) : (r == 2) ? lds_b(rb0 + 3 * 4096) : (r == 3) ? lds_b(rb2 + 3 * 4096) : lds_b(rb1);
    };
    auto mm = [&](auto I, auto C, auto SL) {
        constexpr int i = I, c = C, sl = SL;
        if constexpr (TA && c == 2) acc[i][c] = mfma32(fa[sl], fb[c], acc[i][c]);
        else acc[i][c] = mfma32(fb[c], fa[sl], acc[i][c]);
    };
    auto step = [&](auto PAR) {
        constexpr int par = PAR, X = par ? 3 : 0, Y = par ? 0 : 3;
        sfor<15>([&](auto I) {
            constexpr int idx = I, c = idx / 3, i = idx % 3;
            mm(IC(i), IC(c), IC(X + i));
            if constexpr (idx == 0) fa[Y] = lds_a(ra + 4 * 2048);
            if constexpr (idx == 1) fa[Y + 1] = lds_a(ra + 5 * 2048);
            if constexpr (idx == 2 && TA) fa[Y + 2] = lds_a(ra);
            if constexpr (idx == 4) { bo = (bo == (NB - 1) * BBLK) ? 0u : bo + BBLK; if constexpr (par == 1) ao = (ao == (NA - 1) * ABLK) ? 0u : ao + ABLK; asm volatile("" : "+s"(bo), "+s"(ao)); }
            if constexpr (idx == 6) { rb0 = adB0 + bo; asm volatile("" : "+v"(rb0)); }
            if constexpr (idx == 8 && !TA) { rb1 = adB1 + bo; asm volatile("" : "+v"(rb1)); }
            if constexpr (idx == 10 && !TA) { rb2 = rb0 ^ 32; asm volatile("" : "+v"(rb2)); }
            SB();
        });
        sfor<10>([&](auto I) {
            constexpr int idx = I, c = idx / 2, i = idx % 2;
            mm(IC(3 + i), IC(c), IC(Y + i));
            if constexpr (idx < 3) fa[X + idx] = lds_a(ra + (6 + idx) * 2048);
            if constexpr (idx == 4) { ra = (par == 0) ? (ra ^ 64) : adA0 + ao; asm volatile("" : "+v"(ra)); }
            SB();
        });
        if constexpr (TA) {
            acch[0] = mfma32(fb[1], fa[Y + 2], acch[0]);
            acch[1] = mfma32(fa[Y + 2], fb[2], acch[1]);
        }
        SB();
        bar_lgkm();
        SB();
        sfor<5>([&](auto C) {
            constexpr int c = C;
            mm(IC(5), C, IC(X));
            if constexpr (c < 3) fa[Y + c] = lds_a(ra + (1 + c) * 2048);
            SB();
            mm(IC(6), C, IC(X + 1));
            mm(IC(7), C, IC(X + 2));
            SB();
            rdB(C);
            SB();
        });
    };
    __builtin_amdgcn_s_setprio(1);
    SB();
    bar_lgkm();
    SB();
    sfor<5>([&](auto C) {
        constexpr int c = C;
        if constexpr (c < 3) fa[c] = lds_a(ra + (1 + c) * 2048);
        SB();
        rdB(C);
        SB();
    });
#pragma unroll 1
    for (int s = 0; s < 16; s += 2) {
        step(IC(0));
        step(IC(1));
    }
    __builtin_amdgcn_s_setprio(0);
    constexpr int OST = TA ? 144 : 336;
    const unsigned ow = L_A + tok * OST + g * 8;
    auto cvt4 = [&](f32x4 a) { return u32x2{pk2(a[0], a[1]), pk2(a[2], a[3])}; };
    auto hmul = [&](f32x4 ga, f32x4 ua) {
        f32x4 t;
#pragma unroll
        for (int e = 0; e < 4; ++e) t[e] = fmaxf(ga[e], 0.f);
        return cvt4(t * ua);
    };
    if (TA) {
#pragma unroll
        for (int t = 0; t < 8; ++t) LDS(u32x2, ow + 16 * t * OST + j * 32) = hmul(acc[t][3], acc[t][4]);
        bar_lgkm();
        const unsigned oo = L_B + 16384 + tok * 128 + (((2 * j + (g >> 1)) ^ (tok & 7)) * 16) + (g & 1) * 8;
        const bool par = tok & 1;
        const int da = tok - (4 * g + (tok & 1));
        const bool ca = da >= 0, cb = da >= 2;
        const f32x4 z = {0, 0, 0, 0};
        s16x4 kprev = __builtin_bit_cast(s16x4, cvt4(acch[0]));
        u32x2 vprev = cvt4(acch[1]);
#pragma unroll
        for (int hb = 0; hb < 2; ++hb) {
            s16x4 kf[5], qf[4]; u32x2 vf[5];
            kf[0] = kprev; vf[0] = vprev;
#pragma unroll
            for (int i = 0; i < 4; ++i) {
                kf[i + 1] = __builtin_bit_cast(s16x4, cvt4(acc[4 * hb + i][1]));
                qf[i] = __builtin_bit_cast(s16x4, cvt4(acc[4 * hb + i][0]));
            }
            f32x4 sp[4], sc[4];
#pragma unroll
            for (int i = 0; i < 4; ++i) {
                sp[i] = __builtin_amdgcn_mfma_f32_16x16x16bf16_1k(kf[i], qf[i], z, 0, 0, 0);
                sc[i] = __builtin_amdgcn_mfma_f32_16x16x16bf16_1k(kf[i + 1], qf[i], z, 0, 0, 0);
            }
#pragma unroll
            for (int i = 0; i < 4; ++i) vf[i + 1] = cvt4(acc[4 * hb + i][2]);
            kprev = kf[4]; vprev = vf[4];
            unsigned lw[4];
#pragma unroll
            for (int i = 0; i < 4; ++i) {
                asm volatile("" : "+v"(sp[i]), "+v"(sc[i]));
                const float a0 = par ? sc[i][1] : sc[i][0], a1 = par ? sp[i][1] : sp[i][0];
                const float b0 = par ? sc[i][3] : sc[i][2], b1 = par ? sp[i][3] : sp[i][2];
                float la = ca ? a0 : a1, lb = cb ? b0 : b1;
                if (i == 0 && hb == 0) { if (p == 0) { la = ca ? la : -3.0e38f; lb = cb ? lb : -3.0e38f; } }
                lw[i] = pk2(la, lb);
            }
            tr4(lw);
            float e8[8];
#pragma unroll
            for (int i = 0; i < 4; ++i) { e8[2 * i] = bflo(lw[i]); e8[2 * i + 1] = bfhi(lw[i]); }
            const float mx = fmaxf(fmaxf(fmaxf(e8[0], e8[1]), fmaxf(e8[2], e8[3])), fmaxf(fmaxf(e8[4], e8[5]), fmaxf(e8[6], e8[7])));
            float sum = 0;
#pragma unroll
            for (int i = 0; i < 8; ++i) { e8[i] = __builtin_amdgcn_exp2f((e8[i] - mx) * (0.25f * LOG2E)); sum += e8[i]; }
            const float inv = __builtin_amdgcn_rcpf(sum);
#pragma unroll
            for (int i = 0; i < 4; ++i) lw[i] = pk2(e8[2 * i] * inv, e8[2 * i + 1] * inv);
            tr4(lw);
#pragma unroll
            for (int i = 0; i < 4; ++i) {
                const unsigned pw = lw[i];
                const unsigned wa = par ? (pw << 16) : (pw & 0xffffu), wb = par ? (pw & 0xffff0000u) : (pw >> 16);
                const u32x4 pp = {ca ? 0u : wa, cb ? 0u : wb, ca ? wa : 0u, cb ? wb : 0u};
                const u32x4 vv = {vf[i][0], vf[i][1], vf[i + 1][0], vf[i + 1][1]};
                sp[i] = mfma32(__builtin_bit_cast(bf16x8, vv), __builtin_bit_cast(bf16x8, pp), z);
            }
#pragma unroll
            for (int i = 0; i < 4; ++i) LDS(u32x2, oo + ((4 * hb + i) >> 1) * BBLK + (i & 1) * 2048) = cvt4(sp[i]);
        }
    } else {
        const int jj = j0 - 8, odd = jj & 1;
        const int lo_off = (odd ? 64 : 0) + j * 64;
        const int mx_off = (odd ? 0 : 256) + j * 16;
#pragma unroll
        for (int i = 0; i < 8; ++i)
#pragma unroll
            for (int c = 0; c < 2; ++c) LDS(u32x2, ow + 16 * i * OST + lo_off + c * 32) = hmul(acc[i][c], acc[i][c + 2]);
#pragma unroll
        for (int i = 0; i < 8; i += 2) {
            f32x4 ga, ua;
#pragma unroll
            for (int e = 0; e < 4; ++e) {
                unsigned a = __float_as_uint(acc[i][4][e]), b = __float_as_uint(acc[i + 1][4][e]);
                swap32(a, b);
                ga[e] = __uint_as_float(a); ua[e] = __uint_as_float(b);
            }
            LDS(u32x2, L_A + (16 * (i + (g >> 1)) + tok) * OST + mx_off + (g & 1) * 8) = hmul(ga, ua);
        }
        store_h(a2, false, p, j0);
    }
}

constexpr int a2slot(int st) { return (st & 3) == 3 ? L_B : L_A + (st & 3) * ABLK; }
constexpr int cnt_iv(int t) { return t < 12 ? ((t & 1) ? 7 : 8) : (t == 12 ? 9 : 4); }
constexpr int wait_iv(int t) {
    return t < 2 ? 15 : (t == 14 ? 4 : 0) + cnt_iv(t - 1) + cnt_iv(t);
}

__global__ __launch_bounds__(512, 2) void kfused(const u16* x, const u16* wqkv, const u16* wo, const u16* wg, const u16* wu, const u16* wd,
                                                 u16* a2, u16* out, unsigned* flags) {
    const int w = __builtin_amdgcn_readfirstlane(threadIdx.x >> 6);
    const int b = blockIdx.x, xcd = b & 7, loc = b >> 3;
    if (w == 7) {
        unsigned long long pc;
        asm volatile("s_getpc_b64 %0" : "=s"(pc));
        const int l = threadIdx.x & 63;
        int ln = (l < 32) ? l : 32 + loc * 7 + ((l - 32) % 7);
        ln = ln < CODE_LINES ? ln : 0;
        asm volatile("s_mov_b32 m0, %0\n\ts_nop 0" :: "s"(L_DUMP));
        touch_s((const char*)(pc & ~127ull), (unsigned)(ln * 128));
    }
    const int ci = loc >> 3;
    const int p = (xcd & 1) * 8 + (loc & 7), qd = xcd >> 1;
    const int j0 = (ci < 2) ? 2 * qd + ci : 6 + 2 * qd + ci;
    const int rt = (xcd & 1) * 16 + (xcd >> 1) * 4 + (loc & 3), ct = loc >> 2;
    const int R0 = rt * 64, C0 = ct * 64;
    const bool TA = ci < 2;
    u16* const xn = a2 + (5 << 20);
    unsigned* const inbox = flags + 16384;
    ld_row(L_A + w * 1024, x, p, 16 + 8 * j0 + w);
    if (w == 0) ld_row(L_A + 8192, x, p, j0);
    if (w == 3) touch_v_sc1(inbox + (p * 16 + j0) * 32 + (threadIdx.x & 31), L_FP);
    if (threadIdx.x == 0) LDS(volatile u32x4, L_CNT) = u32x4{0u, 0u, 0u, 0u};
    bar_lgkm();

    const int h = w & 3;
    const int l = threadIdx.x & 63, kq = l >> 3;
    const int lchB = (l & 7) ^ ((((kq >> 1) & 1) | ((h & 1) << 1)) << 1);
    const char *sB0, *sB1, *sB3, *sB4, *gM; unsigned vB0, vB1, st0;
    {
        const int k = 8 * h + kq;
        const int jj = j0 - 8, f0 = 512 + 160 * jj, odd = jj & 1;
        st0 = TA ? 3072u : (unsigned)(F_ * 2);
        vB1 = (unsigned)(k * (F_ * 2) + lchB * 16);
        vB0 = (unsigned)(k * st0 + lchB * 16);
        sB0 = TA ? (const char*)(wqkv + 64 * j0) : (const char*)(wg + f0 + (odd ? 32 : 0));
        sB1 = sB0 + (TA ? 1024 : 128);
        sB3 = TA ? (const char*)(wg + 64 * j0) : (const char*)(wu + f0 + (odd ? 32 : 0));
        sB4 = TA ? (const char*)(wu + 64 * j0) : sB3 + 128;
        const char* m0_ = (const char*)(wg + f0 + (odd ? 0 : 128)), *m1_ = (const char*)(wu + f0 + (odd ? 0 : 128)) - 64;
        gM = (TA ? sB0 + 2048 : (lchB < 4 ? m0_ : m1_)) + vB0;
    }
    const unsigned dB = L_B + h * 1024;
    auto issB = [&](auto SLOT) {
        constexpr int so = SLOT * BBLK;
        setm0<so>(dB); nop1(); dma_s<0>(sB0, vB0);
        setm0<so + 4096>(dB); nop1(); dma_s<0>(sB1, vB0);
        setm0<so + 8192>(dB); nop1(); dma_v(gM);
        setm0<so + 12288>(dB); nop1(); dma_s<0>(sB3, vB1);
        setm0<so + 16384>(dB); nop1(); dma_s<0>(sB4, vB1);
        vB1 += 32 * F_ * 2; vB0 += 32 * st0; gM += 32 * st0;
    };
    if (w >= 4) issB(IC(0));
    ln_exchange(xn, inbox, w, p, j0);

    if (w >= 4) {
        issB(IC(1));
        wait_vm<2>();
        issB(IC(2));
        unsigned* const myP = flags + 32768 + (p * 16 + (l & 15)) * 32 + j0;
        const unsigned* const myE = flags + 512 + (p * 16 + j0) * 32 + (l & 15);
        const unsigned* const pollP = flags + 32768 + ((rt >> 1) * 16 + (rt & 1) * 8 + ct) * 32 + (l & 15);
        unsigned* const echoE = flags + 512 + ((rt >> 1) * 16 + (l & 15)) * 32 + (rt & 1) * 8 + ct;
        unsigned myv = 0, last = 2;
        const char* xs = (const char*)(xn + (size_t)p * 144 * 512);
        unsigned vA[4], vH;
        {
            const int rr = 8 * h + kq;
            vA[0] = (unsigned)((16 + rr) * 1024 + (((l & 7) ^ ((rr >> 1) & 7)) * 16));
            vA[1] = vA[0] + 32768; vA[2] = vA[0] + 65536; vA[3] = vA[0] + 98304;
            const int hr = 4 * h + (kq & 3);
            vH = (unsigned)(hr * 1024 + (((l & 7) ^ ((hr >> 1) & 7)) * 16));
        }
        const unsigned dA = L_A + 2048 + h * 1024, dH = L_A + h * 512;
        auto issA = [&](auto BLK, auto HALF) {
            constexpr int S = BLK, hf = HALF, so = (S % NA) * ABLK - S * 128;
            setm0<so + hf * 8192>(dA); nop1(); dma_s_sc1<S * 128>(xs, vA[2 * hf]);
            setm0<so + hf * 8192 + 4096>(dA); nop1(); dma_s_sc1<S * 128>(xs, vA[2 * hf + 1]);
            if constexpr (hf == 0) { setm0<so>(dH); dma_s32_sc1<S * 128>(xs, vH); }
        };
        const unsigned vW = (unsigned)((8 * h + kq) * 1024 + lchB * 16);
        const char* const wdc = (const char*)(wd + C0);
        const char* const woc = (const char*)(wo + C0);
        auto issW = [&](auto ST) {
            constexpr int st = ST, so = (1 + (st & 3)) * BBLK;
            const char* base = (st < 14) ? wdc + st * 131072 : woc + (st - 14) * 131072;
            sfor<4>([&](auto C) { constexpr int c = C; setm0<so + c * 4096>(dB); nop1(); dma_s<0>(base + c * 32768, vW); });
        };
        auto warm2 = [&](auto R) {
            constexpr int r = R;
            if (h == ((r + 1) & 3)) {
                const int vr = (r * 32 + loc) * 8;
                const char* a = (r < 7) ? (const char*)wd + vr * 1024 : (const char*)wo + (vr - 1792) * 1024;
                touch_v(a + l * 128, L_DUMP);
            }
        };
        bar();
        issA(IC(0), IC(0)); issA(IC(0), IC(1));
        issA(IC(1), IC(0)); issA(IC(1), IC(1));
        wait_bar<5>();
        if (h == 0) touch_v_sc1(myE, L_FE);
        sfor<16>([&](auto T) {
            constexpr int t = T;
            if constexpr (t < 12) issA(IC(t / 2 + 2), IC(t & 1));
            if constexpr (t < 13) issB(IC((t + 3) % NB));
            if constexpr (t >= 12) issW(IC(t - 12));
            if constexpr (t >= W2_AT && t < W2_AT + 9) warm2(IC(t - W2_AT));
            if constexpr (t == 2) {
                if (h == 0) {
                    const unsigned ev = LDS(const volatile unsigned, L_FE + l * 4);
                    unsigned v = 0;
                    while (__ballot(ev == v) != 0ull) v += 4;
                    myv = v;
                    st_sc1(myP, v, 0xffffull);
                }
            }
            if constexpr (t == 6) { if (h == 0) touch_v_sc1(pollP, L_FP); }
            if constexpr (t == 10) {
                if (h == 0) {
                    const unsigned pv = LDS(const volatile unsigned, L_FP + l * 4);
                    st_sc1(echoE, pv, __ballot(!(pv & 3)) & 0xffffull);
                    last = (pv & 3) ? last : pv;
                }
            }
            if constexpr (t == 14) { if (h == 0) touch_v_sc1(myE, L_FE); }
            if constexpr (t < 15) wait_bar<wait_iv(t)>(); else bar();
        });
        unsigned vA2[2];
        { const int row = 8 * h + kq; vA2[0] = (unsigned)(row * (A2LD * 2) + (((l & 7) ^ ((row >> 1) & 7)) * 16)); vA2[1] = vA2[0] + 32 * A2LD * 2; }
        const char* const sa = (const char*)(a2 + (size_t)R0 * A2LD);
        const unsigned dA2 = h * 1024;
        store_h(a2, TA, p, j0);
        unsigned ev = 0, state = 0, shown = 0;
        auto serve = [&](unsigned pv, unsigned long long mask, unsigned ign, bool flush) {
#pragma unroll 1
            for (unsigned spin = 0; spin < SPIN_CAP; ++spin) {
                if (state == 0 && __builtin_amdgcn_readfirstlane(LDS(const volatile unsigned, L_CNT + 4)) == (TA ? 4u : 8u)) state = TA ? 1u : 3u;
                if (state != shown && __ballot(ev != myv) == 0ull) { st_sc1(myP, myv + state, 0xffffull); shown = state; }
                st_sc1(echoE, pv, __ballot(!(pv & 3) && pv != last) & 0xffffull);
                last = (pv & 3) ? last : pv;
                if ((__ballot(((pv - last) | ign) != 3u || (last & 3)) & mask) == 0ull && !(flush && shown != 3u)) break;
                ld2_sc1(pv, pollP, ev, myE);
            }
        };
        if (h == 0) {
            ev = LDS(const volatile unsigned, L_FE + l * 4);
            serve(last, 0xffull, 2u, false);
            if (l == 0) LDS(volatile unsigned, L_CNT + 8) = 1u;
        }
        lds_wait(L_CNT + 12, TA ? 4u : 8u);
        lds_wait(L_CNT + 8, 1u);
        __builtin_amdgcn_s_setprio(2);
        auto issA2 = [&](auto ST) {
            constexpr int st = ST, ko = (st < 14) ? 1024 + st * 256 : (st - 14) * 256, so = a2slot(st);
            const char* sk = sa + ko;
            setm0<so>(dA2); nop1(); dma_s_sc1<0>(sk, vA2[0]);
            setm0<so + 4096>(dA2); nop1(); dma_s_sc1<0>(sk, vA2[1]);
            setm0<so + 8192 - 128>(dA2); nop1(); dma_s_sc1<128>(sk, vA2[0]);
            setm0<so + 12288 - 128>(dA2); nop1(); dma_s_sc1<128>(sk, vA2[1]);
        };
        issA2(IC(0)); issA2(IC(1));
        if (h == 0) serve(last, 0ull, 0u, false);
        wait_vm<4>();
        bar();
        if (h == 0) { serve(last, 0ull, 0u, false); touch_v_sc1(pollP, L_FP); }
        if (TA) {
            const __amdgpu_buffer_rsrc_t a2r = __builtin_amdgcn_make_buffer_rsrc(a2, 0, S_ * A2LD * 2, 0x00020000);
            const int t4 = threadIdx.x & 255, r_ = t4 >> 3;
            const unsigned rd = L_B + 16384 + r_ * 128 + (((t4 & 7) ^ (r_ & 7)) * 16);
            const unsigned go = ((p * 128 + r_) * A2LD + 64 * j0 + (t4 & 7) * 8) * 2;
            u32x4 v[4];
#pragma unroll
            for (int it = 0; it < 4; ++it) v[it] = LDS(const u32x4, rd + it * BBLK);
#pragma unroll
            for (int it = 0; it < 4; ++it) __builtin_amdgcn_raw_buffer_store_b128(v[it], a2r, go, it * 32 * A2LD * 2, 16);
        }
        issA2(IC(2)); issA2(IC(3));
        if (TA) wait_vm<12>(); else wait_vm<8>();
        bar();
        sfor<16>([&](auto ST) {
            constexpr int st = ST;
            if constexpr (st == 0) {
                issW(IC(4));
                if (h == 0) {
                    serve(LDS(const volatile unsigned, L_FP + l * 4), 0xff00ull, 2u, false);
                    if (l == 0) LDS(volatile unsigned, L_CNT + 8) = 2u;
                }
                lds_wait(L_CNT + 8, 2u);
                issA2(IC(4));
            } else if constexpr (st < 14) { issA2(IC(st + 4)); issW(IC(st + 4)); }
            if constexpr (st == 1) { if (h == 0) { state = 3u; serve(last, 0ull, 0u, false); } }
            if constexpr (st == 6) { if (h == 0) touch_v_sc1(pollP, L_FP); }
            if constexpr (st == 9) { if (h == 0) serve(LDS(const volatile unsigned, L_FP + l * 4), 0xffull, 0u, true); }
            wait_bar<(st == 0) ? 12 : (st < 14) ? 16 : (st == 14) ? 8 : 0>();
        });
        return;
    }
    if (w < 3) {
        const int row = 96 + 52 * (loc & 7) + (l < 52 ? l : 51);
#pragma unroll
        for (int i = 0; i < 2; ++i) {
            const int t = 2 * w + i;
            const char* a;
            if (TA) a = (t < 3) ? (const char*)(wqkv + 512 * t + 64 * j0) + row * 3072 : (const char*)((t & 1 ? wg : wu) + 64 * j0) + row * (F_ * 2);
            else a = (const char*)((t & 1 ? wg : wu) + ((512 + 160 * (j0 - 8)) & ~63) + 64 * (t >> 1)) + row * (F_ * 2);
            touch_v(a, L_DUMP);
            if (i == 0) wait_vm<0>();
        }
        bar();
    }
    if (TA) p1_mfma<true>(a2, p, j0); else p1_mfma<false>(a2, p, j0);
    {
        int l2 = threadIdx.x & 63;
        asm volatile("" : "+v"(l2));
        const int g = l2 >> 4, tok = l2 & 15;
        const int wm = w >> 1, wn = w & 1;
        const unsigned a2A = L_A + (32 * wm + tok) * 128 + ((g ^ (tok >> 1)) * 16);
        const unsigned a2B = L_B + (8 * g + (tok >> 2)) * 128 + (((2 * wn) ^ (((tok >> 3) & 1) | ((g & 1) << 1))) * 32) + (l2 & 3) * 8;
        const size_t orow = (size_t)(R0 + 32 * wm + tok) * D_ + C0 + 32 * wn + 4 * g;
        u32x2 xv[2][2];
#pragma unroll
        for (int i = 0; i < 2; ++i)
#pragma unroll
            for (int c = 0; c < 2; ++c) xv[i][c] = *(const u32x2*)(x + orow + i * 16 * D_ + 16 * c);
        f32x4 c2[2][2][2];
#pragma unroll
        for (int a = 0; a < 2; ++a)
#pragma unroll
            for (int i = 0; i < 2; ++i)
#pragma unroll
                for (int c = 0; c < 2; ++c) c2[a][i][c] = f32x4{0, 0, 0, 0};
        bf16x8 ga[2][4][2], gb[2][4][2];
        const unsigned pa0 = a2A, pa1 = a2A ^ 64, pa2 = a2A - L_A, pa3 = pa2 ^ 64;
        const unsigned pb0 = a2B + BBLK, pb1 = pb0 ^ 32, pb2 = pb0 + 2 * BBLK, pb3 = pb2 ^ 32;
        auto rdA = [&](auto SET, auto I, auto Q) {
            constexpr int set = SET, i = I, ks = i >> 1, q = Q;
            const unsigned ab = (q == 3) ? ((ks & 1) ? pa3 : pa2) : ((ks & 1) ? pa1 : pa0);
            ga[set][ks][i & 1] = lds_a(ab + (q == 3 ? 0 : q * ABLK) + (ks >> 1) * 8192 + (i & 1) * 2048);
        };
        auto rdB2 = [&](auto SET, auto I, auto Q) {
            constexpr int set = SET, i = I, ks = i >> 1, q = Q;
            const unsigned bb = (q >= 2) ? ((i & 1) ? pb3 : pb2) : ((i & 1) ? pb1 : pb0);
            gb[set][ks][i & 1] = lds_b(bb + (q & 1) * BBLK + ks * 4096);
        };
        auto rd = [&](auto SET, auto I, auto Q) { rdA(SET, I, Q); rdB2(SET, I, Q); };
        auto stage = [&](auto Q, auto ACC, auto LAST) {
            constexpr int q = Q, set = q & 1, ac = ACC; constexpr bool lastst = LAST;
            if constexpr (!lastst) bar_lgkm();
            SB();
            sfor<16>([&](auto I) {
                constexpr int idx = I, ks = idx >> 2, i = (idx >> 1) & 1, c = idx & 1;
                c2[ac][i][c] = mfma32(gb[set][ks][c], ga[set][ks][i], c2[ac][i][c]);
                if constexpr (!lastst && idx < 12) {
                    constexpr int kk = idx / 3, r3 = idx % 3;
                    if constexpr (r3 == 0) { rdA(IC(set ^ 1), IC(2 * kk), IC((q + 1) & 3)); rdA(IC(set ^ 1), IC(2 * kk + 1), IC((q + 1) & 3)); }
                    else rdB2(IC(set ^ 1), IC(2 * kk + r3 - 1), IC((q + 1) & 3));
                }
                SB();
            });
        };
        typedef std::integral_constant<bool, true> T_; typedef std::integral_constant<bool, false> F_t;
        asm volatile("s_waitcnt vmcnt(0)" : "+v"(xv[0][0]), "+v"(xv[0][1]), "+v"(xv[1][0]), "+v"(xv[1][1]) :: "memory");
        bar_lgkm();
        sfor<8>([&](auto I) { rd(IC(0), I, IC(0)); });
#pragma unroll 1
        for (int it = 0; it < 3; ++it) {
            stage(IC(0), IC(1), F_t{}); stage(IC(1), IC(1), F_t{}); stage(IC(2), IC(1), F_t{}); stage(IC(3), IC(1), F_t{});
        }
        stage(IC(0), IC(1), F_t{}); stage(IC(1), IC(1), F_t{});
        stage(IC(2), IC(0), F_t{}); stage(IC(3), IC(0), F_t{});
        stage(IC(0), IC(0), F_t{}); stage(IC(1), IC(0), T_{});
#pragma unroll
        for (int i = 0; i < 2; ++i)
#pragma unroll
            for (int c = 0; c < 2; ++c) {
                float r[4];
#pragma unroll
                for (int e = 0; e < 4; ++e) {
                    const float d = rbf(c2[1][i][c][e]);
                    const float sg = rbf(d * __builtin_amdgcn_rcpf(1.f + __builtin_amdgcn_exp2f(-d * LOG2E)));
                    const float t = rbf(rbf(c2[0][i][c][e]) * sg);
                    const float xx = (e & 1) ? bfhi(xv[i][c][e >> 1]) : bflo(xv[i][c][e >> 1]);
                    r[e] = xx + t;
                }
                *(u32x2*)(out + orow + i * 16 * D_ + 16 * c) = u32x2{pk2(r[0], r[1]), pk2(r[2], r[3])};
            }
    }
}

extern "C" void kernel_launch(void* const* d_in, const int* in_sizes, int n_in,
                              void* d_out, int out_size, void* d_ws, size_t ws_size,
                              hipStream_t stream) {
    (void)hipFuncSetAttribute((const void*)kfused, hipFuncAttributeMaxDynamicSharedMemorySize, K_LDS);
    kfused<<<256, 512, K_LDS, stream>>>((const u16*)d_in[0], (const u16*)d_in[1], (const u16*)d_in[2], (const u16*)d_in[4], (const u16*)d_in[5],
                                        (const u16*)d_in[6], (u16*)d_ws, (u16*)d_out, (unsigned*)((char*)d_ws + (16 << 20)));
}
```

```cpp
#include <hip/hip_runtime.h>
#include <stdint.h>
#include <utility>

typedef unsigned short u16;
typedef __attribute__((ext_vector_type(8))) __bf16 bf16x8;
typedef __attribute__((ext_vector_type(2))) __bf16 bf16x2;
typedef __attribute__((ext_vector_type(4))) short s16x4;
typedef __attribute__((ext_vector_type(8))) short s16x8;
typedef __attribute__((ext_vector_type(4))) float f32x4;
typedef __attribute__((ext_vector_type(2))) float f32x2;
typedef __attribute__((ext_vector_type(4))) unsigned u32x4;
typedef __attribute__((ext_vector_type(2))) unsigned u32x2;
#define LAS __attribute__((address_space(3)))
#define SB() __builtin_amdgcn_sched_barrier(0)
#define IC(n) std::integral_constant<int, (n)>{}
#define LDS(T, a) (*(LAS T*)(uintptr_t)(a))

constexpr int S_ = 2048, D_ = 512, F_ = 1792;
constexpr int A2LD = 2304;
constexpr float LOG2E = 1.4426950408889634f;

template <int... I, class Fn> __device__ __forceinline__ void sfor_impl(std::integer_sequence<int, I...>, Fn&& f) { (f(std::integral_constant<int, I>{}), ...); }
template <int N, class Fn> __device__ __forceinline__ void sfor(Fn&& f) { sfor_impl(std::make_integer_sequence<int, N>{}, f); }

__device__ __forceinline__ float rbf(float f) { return (float)(__bf16)f; }
__device__ __forceinline__ unsigned pk2(float lo, float hi) {
    bf16x2 v; v[0] = (__bf16)lo; v[1] = (__bf16)hi; return __builtin_bit_cast(unsigned, v);
}
__device__ __forceinline__ float bflo(unsigned u) { return __uint_as_float(u << 16); }
__device__ __forceinline__ float bfhi(unsigned u) { return __uint_as_float(u & 0xffff0000u); }

template <int OFF> __device__ __forceinline__ void setm0(unsigned base) {
    asm volatile("s_add_u32 m0, %0, %1" :: "s"(base), "n"(OFF) : "scc");
}
__device__ __forceinline__ void dma_v(const void* g) { asm volatile("global_load_lds_dwordx4 %0, off" :: "v"(g) : "memory"); }
template <int IO> __device__ __forceinline__ void dma_s(const void* sb, unsigned vo) {
    asm volatile("global_load_lds_dwordx4 %0, %1 offset:%2" :: "v"(vo), "s"(sb), "n"(IO) : "memory");
}
template <int IO> __device__ __forceinline__ void dma_s_sc1(const void* sb, unsigned vo) {
    asm volatile("global_load_lds_dwordx4 %0, %1 offset:%2 sc1" :: "v"(vo), "s"(sb), "n"(IO) : "memory");
}
template <int IO> __device__ __forceinline__ void dma_s32_sc1(const void* sb, unsigned vo) {
    asm volatile("s_mov_b64 exec, %3\n\tglobal_load_lds_dwordx4 %0, %1 offset:%2 sc1\n\ts_mov_b64 exec, -1" :: "v"(vo), "s"(sb), "n"(IO), "s"(0xffffffffull) : "memory");
}
__device__ __forceinline__ void touch_s(const void* sb, unsigned vo) {
    asm volatile("global_load_lds_dword %0, %1" :: "v"(vo), "s"(sb) : "memory");
}
__device__ __forceinline__ unsigned ld_sc1(const unsigned* p) {
    unsigned a; asm volatile("global_load_dword %0, %1, off sc1\n\ts_waitcnt vmcnt(0)" : "=&v"(a) : "v"(p) : "memory"); return a;
}
__device__ __forceinline__ void nop1() { asm volatile("s_nop 0"); }
__device__ __forceinline__ void touch_v(const void* gaddr, unsigned ldsaddr) {
    asm volatile("s_mov_b32 m0, %1\n\ts_nop 0\n\tglobal_load_lds_dword %0, off" :: "v"(gaddr), "s"(ldsaddr) : "memory");
}
__device__ __forceinline__ void touch_v_sc1(const void* gaddr, unsigned ldsaddr) {
    asm volatile("s_mov_b32 m0, %1\n\ts_nop 0\n\tglobal_load_lds_dword %0, off sc1" :: "v"(gaddr), "s"(ldsaddr) : "memory");
}
template <int N> __device__ __forceinline__ void wait_vm() { asm volatile("s_waitcnt vmcnt(%0)" :: "n"(N) : "memory"); }
template <int N> __device__ __forceinline__ void wait_bar() {
    __builtin_amdgcn_s_waitcnt(0xC07F);
    asm volatile("s_waitcnt vmcnt(%0)\n\ts_barrier" :: "n"(N) : "memory");
}
__device__ __forceinline__ void bar_lgkm() { __builtin_amdgcn_s_waitcnt(0xC07F); asm volatile("s_barrier" ::: "memory"); }
__device__ __forceinline__ void bar() { asm volatile("s_barrier" ::: "memory"); }
__device__ __forceinline__ void ld2_sc1(unsigned& a, const unsigned* pa, unsigned& b, const unsigned* pb) {
    asm volatile("global_load_dword %0, %2, off sc1\n\tglobal_load_dword %1, %3, off sc1\n\ts_waitcnt vmcnt(0)"
                 : "=&v"(a), "=&v"(b) : "v"(pa), "v"(pb) : "memory");
}
__device__ __forceinline__ void st_sc1(unsigned* p, unsigned v, unsigned long long mask) {
    asm volatile("s_mov_b64 exec, %2\n\ts_cbranch_execz 1f\n\tglobal_store_dword %0, %1, off sc1\n1:\n\ts_mov_b64 exec, -1"
                 :: "v"(p), "v"(v), "s"(mask) : "memory");
}

__device__ __forceinline__ bf16x8 lds_a(unsigned addr) { return LDS(const bf16x8, addr); }
__device__ __forceinline__ bf16x8 lds_b(unsigned addr) {
    s16x4 lo = __builtin_amdgcn_ds_read_tr16_b64_v4i16((LAS s16x4*)(uintptr_t)addr);
    s16x4 hi = __builtin_amdgcn_ds_read_tr16_b64_v4i16((LAS s16x4*)(uintptr_t)(addr + 512));
    s16x8 r = __builtin_shufflevector(lo, hi, 0, 1, 2, 3, 4, 5, 6, 7);
    return __builtin_bit_cast(bf16x8, r);
}
__device__ __forceinline__ f32x4 mfma32(bf16x8 a, bf16x8 b, f32x4 c) { return __builtin_amdgcn_mfma_f32_16x16x32_bf16(a, b, c, 0, 0, 0); }
__device__ __forceinline__ void swap16(unsigned& a, unsigned& b) { auto r = __builtin_amdgcn_permlane16_swap(a, b, false, false); a = r[0]; b = r[1]; }
__device__ __forceinline__ void swap32(unsigned& a, unsigned& b) { auto r = __builtin_amdgcn_permlane32_swap(a, b, false, false); a = r[0]; b = r[1]; }
__device__ __forceinline__ void tr4(unsigned (&v)[4]) { swap16(v[0], v[1]); swap16(v[2], v[3]); swap32(v[0], v[2]); swap32(v[1], v[3]); }
__device__ __forceinline__ float xadd16(float v) { unsigned a = __float_as_uint(v), b = a; swap16(a, b); return __uint_as_float(a) + __uint_as_float(b); }
template <int CTRL> __device__ __forceinline__ float dadd(float v) {
    return v + __int_as_float(__builtin_amdgcn_update_dpp(0, __float_as_int(v), CTRL, 0xf, 0xf, true));
}
__device__ __forceinline__ float xadd32(float v) { unsigned a = __float_as_uint(v), b = a; swap32(a, b); return __uint_as_float(a) + __uint_as_float(b); }
__device__ __forceinline__ float red8(float v) { return dadd<0x141>(dadd<0x4E>(dadd<0xB1>(v))); }
__device__ __forceinline__ float red64(float v) { return xadd32(xadd16(dadd<0x140>(red8(v)))); }

constexpr int NB = 5, NA = 3;
constexpr int BBLK = 20480, ABLK = 18432;
constexpr int L_B = 0, L_A = NB * BBLK, L_ST = L_A + NA * ABLK;
constexpr int L_CNT = L_ST;
constexpr int L_DUMP = L_ST + 64;
constexpr int L_FE = L_DUMP + 256, L_FP = L_FE + 256;
constexpr int K_LDS = L_FP + 256;
constexpr unsigned SPIN_CAP = 1u << 16;
constexpr int W2_AT = 4;
#define CODE_LINES 228

__device__ __forceinline__ void ld_row(unsigned dst, const u16* x, int p, int ir) {
    int gr = p * 128 - 16 + ir; gr = gr < 0 ? 0 : gr;
    setm0<0>(dst); nop1(); dma_s<0>(x + (size_t)gr * 512, (threadIdx.x & 63) * 16);
}
__device__ __forceinline__ void ln_row(u32x4 v, u16* xn, int p, int ir, bool real) {
    float f[8], s = 0;
#pragma unroll
    for (int e = 0; e < 4; ++e) { const unsigned u = v[e]; f[2 * e] = bflo(u); f[2 * e + 1] = bfhi(u); s += f[2 * e] + f[2 * e + 1]; }
    const float sh = __int_as_float(__builtin_amdgcn_readfirstlane(__float_as_int(f[0])));
    float q = 0;
#pragma unroll
    for (int e = 0; e < 8; ++e) { const float d = f[e] - sh; q = fmaf(d, d, q); }
    const float mean = red64(s) * (1.f / 512.f);
    const float dm = mean - sh;
    const float var = fmaxf(red64(q) * (1.f / 512.f) - dm * dm, 0.f);
    const float sd = rbf(__builtin_amdgcn_sqrtf(rbf(rbf(var) + 1e-5f)));
    const float mb = rbf(mean), rc = __builtin_amdgcn_rcpf(sd);
    float o[8];
#pragma unroll
    for (int e = 0; e < 8; ++e) {
        const float a = rbf(f[e] - mb);
        const float q0 = a * rc;
        o[e] = fmaf(fmaf(-q0, sd, a), rc, q0);
    }
    const u32x4 ov = {pk2(o[0], o[1]), pk2(o[2], o[3]), pk2(o[4], o[5]), pk2(o[6], o[7])};
    const __amdgpu_buffer_rsrc_t xr = __builtin_amdgcn_make_buffer_rsrc(xn, 0, 16 * 148 * 1024, 0x00020000);
    if (real) __builtin_amdgcn_raw_buffer_store_b128(ov, xr, (unsigned)((p * 148 + ir) * 1024 + (threadIdx.x & 63) * 16), 0, 16);
    else asm volatile("" :: "v"(ov));
}
__device__ __forceinline__ void ln_done() {
    wait_vm<0>();
    if ((threadIdx.x & 63) == 0) __hip_atomic_fetch_add((LAS unsigned*)(uintptr_t)L_CNT, 1u, __ATOMIC_RELAXED, __HIP_MEMORY_SCOPE_WORKGROUP);
}

__device__ __forceinline__ void ln_exchange(u16* xn, unsigned* inbox, int w, int p, int j0) {
    const int l = threadIdx.x & 63;
    const unsigned* in = inbox + j0 * 32 + (l & 31);
    unsigned* outp = inbox + (l & 15) * 32 + ((l & 16) ? j0 : 16 + j0);
    unsigned want = 0;
    const int npass = (w == 0) ? 3 : 2;
#pragma unroll 1
    for (int pass = 1; pass < npass; ++pass) {
        if (pass == 1) {
            if (w < 4) wait_vm<0>(); else wait_vm<5>();
            if (w == 3) {
                want = LDS(const volatile unsigned, L_FP + l * 4) + 1u;
                st_sc1(outp, want, 0xffff0000ull);
            }
        }
        const u32x4 v = LDS(const volatile u32x4, L_A + (pass == 2 ? 8 : w) * 1024 + l * 16);
        ln_row(v, xn, p, pass == 2 ? j0 : 16 + 8 * j0 + w, pass != 0);
    }
    ln_done();
    if (w != 3) return;
    unsigned u = ld_sc1(in);
    unsigned spin = 0;
#pragma unroll 1
    while (__builtin_amdgcn_readfirstlane(LDS(const volatile unsigned, L_CNT)) != 8u && ++spin < (SPIN_CAP << 4)) {}
    st_sc1(outp, u, 0xffffull);
    bool go = false;
#pragma unroll 1
    for (spin = 0; spin < SPIN_CAP; ++spin) {
        const unsigned echoed = u;
        u = ld_sc1(in);
        st_sc1(outp, u, __ballot(u != echoed) & 0xffffull);
        if (go) break;
        if ((__ballot(u != want) & 0xffff0000ull) == 0ull) { go = true; bar(); }
    }
    if (!go) bar();
}
__device__ __forceinline__ void store_h(u16* a2, bool TA, int p, int j0) {
    int tid = threadIdx.x;
    asm volatile("" : "+v"(tid));
    const __amdgpu_buffer_rsrc_t a2r = __builtin_amdgcn_make_buffer_rsrc(a2, 0, S_ * A2LD * 2, 0x00020000);
    if (TA) {
        const int t4 = tid & 255;
        const unsigned rd = L_A + (t4 >> 3) * 144 + (t4 & 7) * 16;
        const unsigned go = ((p * 128 + (t4 >> 3)) * A2LD + 512 + 64 * j0 + (t4 & 7) * 8) * 2;
        bar_lgkm();
        u32x4 v[4];
#pragma unroll
        for (int it = 0; it < 4; ++it) v[it] = LDS(const u32x4, rd + it * 32 * 144);
#pragma unroll
        for (int it = 0; it < 4; ++it) __builtin_amdgcn_raw_buffer_store_b128(v[it], a2r, go, it * 32 * A2LD * 2, 16);
    } else {
        unsigned rd[5], go[5];
#pragma unroll
        for (int it = 0; it < 5; ++it) {
            const int id = it * 512 + tid, r_ = id / 20, c_ = id - r_ * 20;
            rd[it] = L_A + r_ * 336 + c_ * 16;
            go[it] = ((p * 128 + r_) * A2LD + 1024 + 160 * (j0 - 8) + c_ * 8) * 2;
        }
        bar_lgkm();
        u32x4 v[5];
#pragma unroll
        for (int it = 0; it < 5; ++it) v[it] = LDS(const u32x4, rd[it]);
#pragma unroll
        for (int it = 0; it < 5; ++it) __builtin_amdgcn_raw_buffer_store_b128(v[it], a2r, go[it], 0, 16);
    }
    if ((tid & 63) == 0) __hip_atomic_fetch_add((LAS unsigned*)(uintptr_t)(L_CNT + 12), 1u, __ATOMIC_RELAXED, __HIP_MEMORY_SCOPE_WORKGROUP);
    wait_vm<0>();
    if ((tid & 63) == 0) __hip_atomic_fetch_add((LAS unsigned*)(uintptr_t)(L_CNT + 4), 1u, __ATOMIC_RELAXED, __HIP_MEMORY_SCOPE_WORKGROUP);
}
__device__ __forceinline__ void lds_wait(int addr, unsigned val) {
    unsigned spin = 0;
#pragma unroll 1
    while (__builtin_amdgcn_readfirstlane(LDS(const volatile unsigned, addr)) != val && ++spin < (SPIN_CAP << 6)) {}
}

template <bool TA>
__device__ __forceinline__ void p1_mfma(u16* a2, int p, int j0) {
    int l = threadIdx.x & 63;
    asm volatile("" : "+v"(l));
    const int j = __builtin_amdgcn_readfirstlane(threadIdx.x >> 6);
    const int g = l >> 4, tok = l & 15;
    const unsigned adA0 = L_A + tok * 128 + ((g ^ (tok >> 1)) * 16);
    const int f2 = ((tok >> 3) & 1) | ((g & 1) << 1);
    const unsigned comB = L_B + (8 * g + (tok >> 2)) * 128;
    unsigned adB0, adB1 = 0;
    {
        const int pp = l & 3;
        if (TA) adB0 = comB + ((j ^ f2) * 32) + pp * 8;
        else {
            adB0 = comB + (j >> 1) * 4096 + (((2 * (j & 1)) ^ f2) * 32) + pp * 8;
            adB1 = comB + 2 * 4096 + ((((pp >> 1) * 2 + (j >> 1)) ^ f2) * 32) + (j & 1) * 16 + (pp & 1) * 8;
        }
    }
    f32x4 acc[8][5], acch[2];
#pragma unroll
    for (int i = 0; i < 8; ++i)
#pragma unroll
        for (int c = 0; c < 5; ++c) { acc[i][c] = f32x4{0, 0, 0, 0}; asm volatile("" : "+v"(acc[i][c])); }
    acch[0] = acch[1] = f32x4{0, 0, 0, 0};
    asm volatile("" : "+v"(acch[0]), "+v"(acch[1]));
    bf16x8 fa[6], fb[5];
    unsigned ra = adA0, rb0 = adB0, rb1 = adB1, rb2 = adB0 ^ 32;
    unsigned bo = 0, ao = 0;
    auto rdB = [&](auto R) {
        constexpr int r = R;
        if constexpr (TA) fb[r] = lds_b(rb0 + r * 4096);
        else fb[r] = (r == 0) ? lds_b(rb0) : (r == 1) ? lds_b(rb2) : (r == 2) ? lds_b(rb0 + 3 * 4096) : (r == 3) ? lds_b(rb2 + 3 * 4096) : lds_b(rb1);
    };
    auto mm = [&](auto I, auto C, auto SL) {
        constexpr int i = I, c = C, sl = SL;
        if constexpr (TA && c == 2) acc[i][c] = mfma32(fa[sl], fb[c], acc[i][c]);
        else acc[i][c] = mfma32(fb[c], fa[sl], acc[i][c]);
    };
    auto step = [&](auto PAR) {
        constexpr int par = PAR, X = par ? 3 : 0, Y = par ? 0 : 3;
        sfor<15>([&](auto I) {
            constexpr int idx = I, c = idx / 3, i = idx % 3;
            mm(IC(i), IC(c), IC(X + i));
            if constexpr (idx == 0) fa[Y] = lds_a(ra + 4 * 2048);
            if constexpr (idx == 1) fa[Y + 1] = lds_a(ra + 5 * 2048);
            if constexpr (idx == 2 && TA) fa[Y + 2] = lds_a(ra);
            if constexpr (idx == 4) { bo = (bo == (NB - 1) * BBLK) ? 0u : bo + BBLK; if constexpr (par == 1) ao = (ao == (NA - 1) * ABLK) ? 0u : ao + ABLK; asm volatile("" : "+s"(bo), "+s"(ao)); }
            if constexpr (idx == 6) { rb0 = adB0 + bo; asm volatile("" : "+v"(rb0)); }
            if constexpr (idx == 8 && !TA) { rb1 = adB1 + bo; asm volatile("" : "+v"(rb1)); }
            if constexpr (idx == 10 && !TA) { rb2 = rb0 ^ 32; asm volatile("" : "+v"(rb2)); }
            SB();
        });
        sfor<10>([&](auto I) {
            constexpr int idx = I, c = idx / 2, i = idx % 2;
            mm(IC(3 + i), IC(c), IC(Y + i));
            if constexpr (idx < 3) fa[X + idx] = lds_a(ra + (6 + idx) * 2048);
            if constexpr (idx == 4) { ra = (par == 0) ? (ra ^ 64) : adA0 + ao; asm volatile("" : "+v"(ra)); }
            SB();
        });
        if constexpr (TA) {
            acch[0] = mfma32(fb[1], fa[Y + 2], acch[0]);
            acch[1] = mfma32(fa[Y + 2], fb[2], acch[1]);
        }
        SB();
        bar_lgkm();
        SB();
        sfor<5>([&](auto C) {
            constexpr int c = C;
            mm(IC(5), C, IC(X));
            if constexpr (c < 3) fa[Y + c] = lds_a(ra + (1 + c) * 2048);
            SB();
            mm(IC(6), C, IC(X + 1));
            mm(IC(7), C, IC(X + 2));
            SB();
            rdB(C);
            SB();
        });
    };
    __builtin_amdgcn_s_setprio(1);
    SB();
    bar_lgkm();
    SB();
    sfor<5>([&](auto C) {
        constexpr int c = C;
        if constexpr (c < 3) fa[c] = lds_a(ra + (1 + c) * 2048);
        SB();
        rdB(C);
        SB();
    });
#pragma unroll 1
    for (int s = 0; s < 16; s += 2) {
        step(IC(0));
        step(IC(1));
    }
    __builtin_amdgcn_s_setprio(0);
    constexpr int OST = TA ? 144 : 336;
    const unsigned ow = L_A + tok * OST + g * 8;
    auto cvt4 = [&](f32x4 a) { return u32x2{pk2(a[0], a[1]), pk2(a[2], a[3])}; };
    auto hmul = [&](f32x4 ga, f32x4 ua) {
        f32x4 t;
#pragma unroll
        for (int e = 0; e < 4; ++e) t[e] = fmaxf(ga[e], 0.f);
        return cvt4(t * ua);
    };
    if (TA) {
#pragma unroll
        for (int t = 0; t < 8; ++t) LDS(u32x2, ow + 16 * t * OST + j * 32) = hmul(acc[t][3], acc[t][4]);
        bar_lgkm();
        const unsigned oo = L_B + 16384 + tok * 128 + (((2 * j + (g >> 1)) ^ (tok & 7)) * 16) + (g & 1) * 8;
        const bool par = tok & 1;
        const int da = tok - (4 * g + (tok & 1));
        const bool ca = da >= 0, cb = da >= 2;
        const f32x4 z = {0, 0, 0, 0};
        s16x4 kprev = __builtin_bit_cast(s16x4, cvt4(acch[0]));
        u32x2 vprev = cvt4(acch[1]);
#pragma unroll
        for (int hb = 0; hb < 2; ++hb) {
            s16x4 kf[5], qf[4]; u32x2 vf[5];
            kf[0] = kprev; vf[0] = vprev;
#pragma unroll
            for (int i = 0; i < 4; ++i) {
                kf[i + 1] = __builtin_bit_cast(s16x4, cvt4(acc[4 * hb + i][1]));
                qf[i] = __builtin_bit_cast(s16x4, cvt4(acc[4 * hb + i][0]));
            }
            f32x4 sp[4], sc[4];
#pragma unroll
            for (int i = 0; i < 4; ++i) {
                sp[i] = __builtin_amdgcn_mfma_f32_16x16x16bf16_1k(kf[i], qf[i], z, 0, 0, 0);
                sc[i] = __builtin_amdgcn_mfma_f32_16x16x16bf16_1k(kf[i + 1], qf[i], z, 0, 0, 0);
            }
#pragma unroll
            for (int i = 0; i < 4; ++i) vf[i + 1] = cvt4(acc[4 * hb + i][2]);
            kprev = kf[4]; vprev = vf[4];
            unsigned lw[4];
#pragma unroll
            for (int i = 0; i < 4; ++i) {
                asm volatile("" : "+v"(sp[i]), "+v"(sc[i]));
                const float a0 = par ? sc[i][1] : sc[i][0], a1 = par ? sp[i][1] : sp[i][0];
                const float b0 = par ? sc[i][3] : sc[i][2], b1 = par ? sp[i][3] : sp[i][2];
                float la = ca ? a0 : a1, lb = cb ? b0 : b1;
                if (i == 0 && hb == 0) { if (p == 0) { la = ca ? la : -3.0e38f; lb = cb ? lb : -3.0e38f; } }
                lw[i] = pk2(la, lb);
            }
            tr4(lw);
            float e8[8];
#pragma unroll
            for (int i = 0; i < 4; ++i) { e8[2 * i] = bflo(lw[i]); e8[2 * i + 1] = bfhi(lw[i]); }
            const float mx = fmaxf(fmaxf(fmaxf(e8[0], e8[1]), fmaxf(e8[2], e8[3])), fmaxf(fmaxf(e8[4], e8[5]), fmaxf(e8[6], e8[7])));
            float sum = 0;
#pragma unroll
            for (int i = 0; i < 8; ++i) { e8[i] = __builtin_amdgcn_exp2f((e8[i] - mx) * (0.25f * LOG2E)); sum += e8[i]; }
            const float inv = __builtin_amdgcn_rcpf(sum);
#pragma unroll
            for (int i = 0; i < 4; ++i) lw[i] = pk2(e8[2 * i] * inv, e8[2 * i + 1] * inv);
            tr4(lw);
#pragma unroll
            for (int i = 0; i < 4; ++i) {
                const unsigned pw = lw[i];
                const unsigned wa = par ? (pw << 16) : (pw & 0xffffu), wb = par ? (pw & 0xffff0000u) : (pw >> 16);
                const u32x4 pp = {ca ? 0u : wa, cb ? 0u : wb, ca ? wa : 0u, cb ? wb : 0u};
                const u32x4 vv = {vf[i][0], vf[i][1], vf[i + 1][0], vf[i + 1][1]};
                sp[i] = mfma32(__builtin_bit_cast(bf16x8, vv), __builtin_bit_cast(bf16x8, pp), z);
            }
#pragma unroll
            for (int i = 0; i < 4; ++i) LDS(u32x2, oo + ((4 * hb + i) >> 1) * BBLK + (i & 1) * 2048) = cvt4(sp[i]);
        }
    } else {
        const int jj = j0 - 8, odd = jj & 1;
        const int lo_off = (odd ? 64 : 0) + j * 64;
        const int mx_off = (odd ? 0 : 256) + j * 16;
#pragma unroll
        for (int i = 0; i < 8; ++i)
#pragma unroll
            for (int c = 0; c < 2; ++c) LDS(u32x2, ow + 16 * i * OST + lo_off + c * 32) = hmul(acc[i][c], acc[i][c + 2]);
#pragma unroll
        for (int i = 0; i < 8; i += 2) {
            f32x4 ga, ua;
#pragma unroll
            for (int e = 0; e < 4; ++e) {
                unsigned a = __float_as_uint(acc[i][4][e]), b = __float_as_uint(acc[i + 1][4][e]);
                swap32(a, b);
                ga[e] = __uint_as_float(a); ua[e] = __uint_as_float(b);
            }
            LDS(u32x2, L_A + (16 * (i + (g >> 1)) + tok) * OST + mx_off + (g & 1) * 8) = hmul(ga, ua);
        }
        store_h(a2, false, p, j0);
    }
}

constexpr int a2slot(int st) { return (st & 3) == 3 ? L_B : L_A + (st & 3) * ABLK; }
constexpr int cnt_iv(int t) { return t < 12 ? ((t & 1) ? 7 : 8) : (t == 12 ? 9 : 4); }
constexpr int wait_iv(int t) {
    return t < 2 ? 15 : (t == 14 ? 4 : 0) + cnt_iv(t - 1) + cnt_iv(t);
}

__global__ __launch_bounds__(512, 2) void kfused(const u16* x, const u16* wqkv, const u16* wo, const u16* wg, const u16* wu, const u16* wd,
                                                 u16* a2, u16* out, unsigned* flags) {
    const int w = __builtin_amdgcn_readfirstlane(threadIdx.x >> 6);
    const int b = blockIdx.x, xcd = b & 7, loc = b >> 3;
    if (w == 7) {
        unsigned long long pc;
        asm volatile("s_getpc_b64 %0" : "=s"(pc));
        const int l = threadIdx.x & 63;
        int ln = (l < 32) ? l : 32 + loc * 7 + ((l - 32) % 7);
        ln = ln < CODE_LINES ? ln : 0;
        asm volatile("s_mov_b32 m0, %0\n\ts_nop 0" :: "s"(L_DUMP));
        touch_s((const char*)(pc & ~127ull), (unsigned)(ln * 128));
    }
    const int ci = loc >> 3;
    const int p = (xcd & 1) * 8 + (loc & 7), qd = xcd >> 1;
    const int j0 = (ci < 2) ? 2 * qd + ci : 6 + 2 * qd + ci;
    const int rt = (xcd & 1) * 16 + (xcd >> 1) * 4 + (loc & 3), ct = loc >> 2;
    const int R0 = rt * 64, C0 = ct * 64;
    const bool TA = ci < 2;
    u16* const xn = a2 + (5 << 20);
    unsigned* const inbox = (unsigned*)(xn + (size_t)(p * 148 + 144) * 512);
    ld_row(L_A + w * 1024, x, p, 16 + 8 * j0 + w);
    if (w == 0) ld_row(L_A + 8192, x, p, j0);
    if (w == 3) touch_v_sc1(inbox + j0 * 32 + (threadIdx.x & 31), L_FP);
    if (threadIdx.x == 0) LDS(volatile u32x4, L_CNT) = u32x4{0u, 0u, 0u, 0u};
    bar_lgkm();

    const int h = w & 3;
    const int l = threadIdx.x & 63, kq = l >> 3;
    const int lchB = (l & 7) ^ ((((kq >> 1) & 1) | ((h & 1) << 1)) << 1);
    const char *sB0, *sB1, *sB3, *sB4, *gM; unsigned vB0, vB1, st0;
    {
        const int k = 8 * h + kq;
        const int jj = j0 - 8, f0 = 512 + 160 * jj, odd = jj & 1;
        st0 = TA ? 3072u : (unsigned)(F_ * 2);
        vB1 = (unsigned)(k * (F_ * 2) + lchB * 16);
        vB0 = (unsigned)(k * st0 + lchB * 16);
        sB0 = TA ? (const char*)(wqkv + 64 * j0) : (const char*)(wg + f0 + (odd ? 32 : 0));
        sB1 = sB0 + (TA ? 1024 : 128);
        sB3 = TA ? (const char*)(wg + 64 * j0) : (const char*)(wu + f0 + (odd ? 32 : 0));
        sB4 = TA ? (const char*)(wu + 64 * j0) : sB3 + 128;
        const char* m0_ = (const char*)(wg + f0 + (odd ? 0 : 128)), *m1_ = (const char*)(wu + f0 + (odd ? 0 : 128)) - 64;
        gM = (TA ? sB0 + 2048 : (lchB < 4 ? m0_ : m1_)) + vB0;
    }
    const unsigned dB = L_B + h * 1024;
    auto issB = [&](auto SLOT) {
        constexpr int so = SLOT * BBLK;
        setm0<so>(dB); nop1(); dma_s<0>(sB0, vB0);
        setm0<so + 4096>(dB); nop1(); dma_s<0>(sB1, vB0);
        setm0<so + 8192>(dB); nop1(); dma_v(gM);
        setm0<so + 12288>(dB); nop1(); dma_s<0>(sB3, vB1);
        setm0<so + 16384>(dB); nop1(); dma_s<0>(sB4, vB1);
        vB1 += 32 * F_ * 2; vB0 += 32 * st0; gM += 32 * st0;
    };
    if (w >= 4) issB(IC(0));
    ln_exchange(xn, inbox, w, p, j0);

    if (w >= 4) {
        issB(IC(1));
        wait_vm<2>();
        issB(IC(2));
        unsigned* const myP = flags + 32768 + (p * 16 + (l & 15)) * 32 + j0;
        const unsigned* const myE = flags + 512 + (p * 16 + j0) * 32 + (l & 15);
        const unsigned* const pollP = flags + 32768 + ((rt >> 1) * 16 + (rt & 1) * 8 + ct) * 32 + (l & 15);
        unsigned* const echoE = flags + 512 + ((rt >> 1) * 16 + (l & 15)) * 32 + (rt & 1) * 8 + ct;
        unsigned myv = 0, last = 2;
        const char* xs = (const char*)(xn + (size_t)p * 148 * 512);
        unsigned vA[4], vH;
        {
            const int rr = 8 * h + kq;
            vA[0] = (unsigned)((16 + rr) * 1024 + (((l & 7) ^ ((rr >> 1) & 7)) * 16));
            vA[1] = vA[0] + 32768; vA[2] = vA[0] + 65536; vA[3] = vA[0] + 98304;
            const int hr = 4 * h + (kq & 3);
            vH = (unsigned)(hr * 1024 + (((l & 7) ^ ((hr >> 1) & 7)) * 16));
        }
        const unsigned dA = L_A + 2048 + h * 1024, dH = L_A + h * 512;
        auto issA = [&](auto BLK, auto HALF) {
            constexpr int S = BLK, hf = HALF, so = (S % NA) * ABLK - S * 128;
            setm0<so + hf * 8192>(dA); nop1(); dma_s_sc1<S * 128>(xs, vA[2 * hf]);
            setm0<so + hf * 8192 + 4096>(dA); nop1(); dma_s_sc1<S * 128>(xs, vA[2 * hf + 1]);
            if constexpr (hf == 0) { setm0<so>(dH); dma_s32_sc1<S * 128>(xs, vH); }
        };
        const unsigned vW = (unsigned)((8 * h + kq) * 1024 + lchB * 16);
        const char* const wdc = (const char*)(wd + C0);
        const char* const woc = (const char*)(wo + C0);
        auto issW = [&](auto ST) {
            constexpr int st = ST, so = (1 + (st & 3)) * BBLK;
            const char* base = (st < 14) ? wdc + st * 131072 : woc + (st - 14) * 131072;
            sfor<4>([&](auto C) { constexpr int c = C; setm0<so + c * 4096>(dB); nop1(); dma_s<0>(base + c * 32768, vW); });
        };
        auto warm2 = [&](auto R) {
            constexpr int r = R;
            if (h == ((r + 1) & 3)) {
                const int vr = (r * 32 + loc) * 8;
                const char* a = (r < 7) ? (const char*)wd + vr * 1024 : (const char*)wo + (vr - 1792) * 1024;
                touch_v(a + l * 128, L_DUMP);
            }
        };
        bar();
        issA(IC(0), IC(0)); issA(IC(0), IC(1));
        issA(IC(1), IC(0)); issA(IC(1), IC(1));
        wait_bar<5>();
        if (h == 0) touch_v_sc1(myE, L_FE);
        sfor<16>([&](auto T) {
            constexpr int t = T;
            if constexpr (t < 12) issA(IC(t / 2 + 2), IC(t & 1));
            if constexpr (t < 13) issB(IC((t + 3) % NB));
            if constexpr (t >= 12) issW(IC(t - 12));
            if constexpr (t >= W2_AT && t < W2_AT + 9) warm2(IC(t - W2_AT));
            if constexpr (t == 2) {
                if (h == 0) {
                    const unsigned ev = LDS(const volatile unsigned, L_FE + l * 4);
                    unsigned v = 0;
                    while (__ballot(ev == v) != 0ull) v += 4;
                    myv = v;
                    st_sc1(myP, v, 0xffffull);
                }
            }
            if constexpr (t == 6) { if (h == 0) touch_v_sc1(pollP, L_FP); }
            if constexpr (t == 10) {
                if (h == 0) {
                    const unsigned pv = LDS(const volatile unsigned, L_FP + l * 4);
                    st_sc1(echoE, pv, __ballot(!(pv & 3)) & 0xffffull);
                    last = (pv & 3) ? last : pv;
                }
            }
            if constexpr (t == 14) { if (h == 0) touch_v_sc1(myE, L_FE); }
            if constexpr (t < 15) wait_bar<wait_iv(t)>(); else bar();
        });
        unsigned vA2[2];
        { const int row = 8 * h + kq; vA2[0] = (unsigned)(row * (A2LD * 2) + (((l & 7) ^ ((row >> 1) & 7)) * 16)); vA2[1] = vA2[0] + 32 * A2LD * 2; }
        const char* const sa = (const char*)(a2 + (size_t)R0 * A2LD);
        const unsigned dA2 = h * 1024;
        store_h(a2, TA, p, j0);
        unsigned ev = 0, state = 0, shown = 0;
        auto serve = [&](unsigned pv, unsigned long long mask, unsigned ign, bool flush) {
#pragma unroll 1
            for (unsigned spin = 0; spin < SPIN_CAP; ++spin) {
                if (state == 0 && __builtin_amdgcn_readfirstlane(LDS(const volatile unsigned, L_CNT + 4)) == (TA ? 4u : 8u)) state = TA ? 1u : 3u;
                if (state != shown && __ballot(ev != myv) == 0ull) { st_sc1(myP, myv + state, 0xffffull); shown = state; }
                st_sc1(echoE, pv, __ballot(!(pv & 3) && pv != last) & 0xffffull);
                last = (pv & 3) ? last : pv;
                if ((__ballot(((pv - last) | ign) != 3u || (last & 3)) & mask) == 0ull && !(flush && shown != 3u)) break;
                ld2_sc1(pv, pollP, ev, myE);
            }
        };
        if (h == 0) {
            ev = LDS(const volatile unsigned, L_FE + l * 4);
            serve(last, 0xffull, 2u, false);
            if (l == 0) LDS(volatile unsigned, L_CNT + 8) = 1u;
        }
        lds_wait(L_CNT + 12, TA ? 4u : 8u);
        lds_wait(L_CNT + 8, 1u);
        __builtin_amdgcn_s_setprio(2);
        auto issA2 = [&](auto ST) {
            constexpr int st = ST, ko = (st < 14) ? 1024 + st * 256 : (st - 14) * 256, so = a2slot(st);
            const char* sk = sa + ko;
            setm0<so>(dA2); nop1(); dma_s_sc1<0>(sk, vA2[0]);
            setm0<so + 4096>(dA2); nop1(); dma_s_sc1<0>(sk, vA2[1]);
            setm0<so + 8192 - 128>(dA2); nop1(); dma_s_sc1<128>(sk, vA2[0]);
            setm0<so + 12288 - 128>(dA2); nop1(); dma_s_sc1<128>(sk, vA2[1]);
        };
        issA2(IC(0)); issA2(IC(1));
        if (h == 0) serve(last, 0ull, 0u, false);
        wait_vm<4>();
        bar();
        if (h == 0) { serve(last, 0ull, 0u, false); touch_v_sc1(pollP, L_FP); }
        if (TA) {
            const __amdgpu_buffer_rsrc_t a2r = __builtin_amdgcn_make_buffer_rsrc(a2, 0, S_ * A2LD * 2, 0x00020000);
            const int t4 = threadIdx.x & 255, r_ = t4 >> 3;
            const unsigned rd = L_B + 16384 + r_ * 128 + (((t4 & 7) ^ (r_ & 7)) * 16);
            const unsigned go = ((p * 128 + r_) * A2LD + 64 * j0 + (t4 & 7) * 8) * 2;
            u32x4 v[4];
#pragma unroll
            for (int it = 0; it < 4; ++it) v[it] = LDS(const u32x4, rd + it * BBLK);
#pragma unroll
            for (int it = 0; it < 4; ++it) __builtin_amdgcn_raw_buffer_store_b128(v[it], a2r, go, it * 32 * A2LD * 2, 16);
        }
        issA2(IC(2)); issA2(IC(3));
        if (TA) wait_vm<12>(); else wait_vm<8>();
        bar();
        sfor<16>([&](auto ST) {
            constexpr int st = ST;
            if constexpr (st == 0) {
                issW(IC(4));
                if (h == 0) {
                    serve(LDS(const volatile unsigned, L_FP + l * 4), 0xff00ull, 2u, false);
                    if (l == 0) LDS(volatile unsigned, L_CNT + 8) = 2u;
                }
                lds_wait(L_CNT + 8, 2u);
                issA2(IC(4));
            } else if constexpr (st < 14) { issA2(IC(st + 4)); issW(IC(st + 4)); }
            if constexpr (st == 1) { if (h == 0) { state = 3u; serve(last, 0ull, 0u, false); } }
            if constexpr (st == 6) { if (h == 0) touch_v_sc1(pollP, L_FP); }
            if constexpr (st == 9) { if (h == 0) serve(LDS(const volatile unsigned, L_FP + l * 4), 0xffull, 0u, true); }
            wait_bar<(st == 0) ? 12 : (st < 14) ? 16 : (st == 14) ? 8 : 0>();
        });
        return;
    }
    if (w < 3) {
        const int row = 96 + 52 * (loc & 7) + (l < 52 ? l : 51);
#pragma unroll
        for (int i = 0; i < 2; ++i) {
            const int t = 2 * w + i;
            const char* a;
            if (TA) a = (t < 3) ? (const char*)(wqkv + 512 * t + 64 * j0) + row * 3072 : (const char*)((t & 1 ? wg : wu) + 64 * j0) + row * (F_ * 2);
            else a = (const char*)((t & 1 ? wg : wu) + ((512 + 160 * (j0 - 8)) & ~63) + 64 * (t >> 1)) + row * (F_ * 2);
            touch_v(a, L_DUMP);
            if (i == 0) wait_vm<0>();
        }
        bar();
    }
    if (TA) p1_mfma<true>(a2, p, j0); else p1_mfma<false>(a2, p, j0);
    {
        int l2 = threadIdx.x & 63;
        asm volatile("" : "+v"(l2));
        const int g = l2 >> 4, tok = l2 & 15;
        const int wm = w >> 1, wn = w & 1;
        const unsigned a2A = L_A + (32 * wm + tok) * 128 + ((g ^ (tok >> 1)) * 16);
        const unsigned a2B = L_B + (8 * g + (tok >> 2)) * 128 + (((2 * wn) ^ (((tok >> 3) & 1) | ((g & 1) << 1))) * 32) + (l2 & 3) * 8;
        const size_t orow = (size_t)(R0 + 32 * wm + tok) * D_ + C0 + 32 * wn + 4 * g;
        u32x2 xv[2][2];
#pragma unroll
        for (int i = 0; i < 2; ++i)
#pragma unroll
            for (int c = 0; c < 2; ++c) xv[i][c] = *(const u32x2*)(x + orow + i * 16 * D_ + 16 * c);
        f32x4 c2[2][2][2];
#pragma unroll
        for (int a = 0; a < 2; ++a)
#pragma unroll
            for (int i = 0; i < 2; ++i)
#pragma unroll
                for (int c = 0; c < 2; ++c) c2[a][i][c] = f32x4{0, 0, 0, 0};
        bf16x8 ga[2][4][2], gb[2][4][2];
        const unsigned pa0 = a2A, pa1 = a2A ^ 64, pa2 = a2A - L_A, pa3 = pa2 ^ 64;
        const unsigned pb0 = a2B + BBLK, pb1 = pb0 ^ 32, pb2 = pb0 + 2 * BBLK, pb3 = pb2 ^ 32;
        auto rdA = [&](auto SET, auto I, auto Q) {
            constexpr int set = SET, i = I, ks = i >> 1, q = Q;
            const unsigned ab = (q == 3) ? ((ks & 1) ? pa3 : pa2) : ((ks & 1) ? pa1 : pa0);
            ga[set][ks][i & 1] = lds_a(ab + (q == 3 ? 0 : q * ABLK) + (ks >> 1) * 8192 + (i & 1) * 2048);
        };
        auto rdB2 = [&](auto SET, auto I, auto Q) {
            constexpr int set = SET, i = I, ks = i >> 1, q = Q;
            const unsigned bb = (q >= 2) ? ((i & 1) ? pb3 : pb2) : ((i & 1) ? pb1 : pb0);
            gb[set][ks][i & 1] = lds_b(bb + (q & 1) * BBLK + ks * 4096);
        };
        auto rd = [&](auto SET, auto I, auto Q) { rdA(SET, I, Q); rdB2(SET, I, Q); };
        auto stage = [&](auto Q, auto ACC, auto LAST) {
            constexpr int q = Q, set = q & 1, ac = ACC; constexpr bool lastst = LAST;
            if constexpr (!lastst) bar_lgkm();
            SB();
            sfor<16>([&](auto I) {
                constexpr int idx = I, ks = idx >> 2, i = (idx >> 1) & 1, c = idx & 1;
                c2[ac][i][c] = mfma32(gb[set][ks][c], ga[set][ks][i], c2[ac][i][c]);
                if constexpr (!lastst && idx < 12) {
                    constexpr int kk = idx / 3, r3 = idx % 3;
                    if constexpr (r3 == 0) { rdA(IC(set ^ 1), IC(2 * kk), IC((q + 1) & 3)); rdA(IC(set ^ 1), IC(2 * kk + 1), IC((q + 1) & 3)); }
                    else rdB2(IC(set ^ 1), IC(2 * kk + r3 - 1), IC((q + 1) & 3));
                }
                SB();
            });
        };
        typedef std::integral_constant<bool, true> T_; typedef std::integral_constant<bool, false> F_t;
        asm volatile("s_waitcnt vmcnt(0)" : "+v"(xv[0][0]), "+v"(xv[0][1]), "+v"(xv[1][0]), "+v"(xv[1][1]) :: "memory");
        bar_lgkm();
        sfor<8>([&](auto I) { rd(IC(0), I, IC(0)); });
#pragma unroll 1
        for (int it = 0; it < 3; ++it) {
            stage(IC(0), IC(1), F_t{}); stage(IC(1), IC(1), F_t{}); stage(IC(2), IC(1), F_t{}); stage(IC(3), IC(1), F_t{});
        }
        stage(IC(0), IC(1), F_t{}); stage(IC(1), IC(1), F_t{});
        stage(IC(2), IC(0), F_t{}); stage(IC(3), IC(0), F_t{});
        stage(IC(0), IC(0), F_t{}); stage(IC(1), IC(0), T_{});
#pragma unroll
        for (int i = 0; i < 2; ++i)
#pragma unroll
            for (int c = 0; c < 2; ++c) {
                float r[4];
#pragma unroll
                for (int e = 0; e < 4; ++e) {
                    const float d = rbf(c2[1][i][c][e]);
                    const float sg = rbf(d * __builtin_amdgcn_rcpf(1.f + __builtin_amdgcn_exp2f(-d * LOG2E)));
                    const float t = rbf(rbf(c2[0][i][c][e]) * sg);
                    const float xx = (e & 1) ? bfhi(xv[i][c][e >> 1]) : bflo(xv[i][c][e >> 1]);
                    r[e] = xx + t;
                }
                *(u32x2*)(out + orow + i * 16 * D_ + 16 * c) = u32x2{pk2(r[0], r[1]), pk2(r[2], r[3])};
            }
    }
}

extern "C" void kernel_launch(void* const* d_in, const int* in_sizes, int n_in,
                              void* d_out, int out_size, void* d_ws, size_t ws_size,
                              hipStream_t stream) {
    (void)hipFuncSetAttribute((const void*)kfused, hipFuncAttributeMaxDynamicSharedMemorySize, K_LDS);
    kfused<<<256, 512, K_LDS, stream>>>((const u16*)d_in[0], (const u16*)d_in[1], (const u16*)d_in[2], (const u16*)d_in[4], (const u16*)d_in[5],
                                        (const u16*)d_in[6], (u16*)d_ws, (u16*)d_out, (unsigned*)((char*)d_ws + (16 << 20)));
}
```

```cpp
#include <hip/hip_runtime.h>
#include <stdint.h>
#include <utility>

typedef unsigned short u16;
typedef __attribute__((ext_vector_type(8))) __bf16 bf16x8;
typedef __attribute__((ext_vector_type(2))) __bf16 bf16x2;
typedef __attribute__((ext_vector_type(4))) short s16x4;
typedef __attribute__((ext_vector_type(8))) short s16x8;
typedef __attribute__((ext_vector_type(4))) float f32x4;
typedef __attribute__((ext_vector_type(2))) float f32x2;
typedef __attribute__((ext_vector_type(4))) unsigned u32x4;
typedef __attribute__((ext_vector_type(2))) unsigned u32x2;
#define LAS __attribute__((address_space(3)))
#define SB() __builtin_amdgcn_sched_barrier(0)
#define IC(n) std::integral_constant<int, (n)>{}
#define LDS(T, a) (*(LAS T*)(uintptr_t)(a))

constexpr int S_ = 2048, D_ = 512, F_ = 1792;
constexpr int A2LD = 2304;
constexpr float LOG2E = 1.4426950408889634f;

template <int... I, class Fn> __device__ __forceinline__ void sfor_impl(std::integer_sequence<int, I...>, Fn&& f) { (f(std::integral_constant<int, I>{}), ...); }
template <int N, class Fn> __device__ __forceinline__ void sfor(Fn&& f) { sfor_impl(std::make_integer_sequence<int, N>{}, f); }

__device__ __forceinline__ float rbf(float f) { return (float)(__bf16)f; }
__device__ __forceinline__ unsigned pk2(float lo, float hi) {
    bf16x2 v; v[0] = (__bf16)lo; v[1] = (__bf16)hi; return __builtin_bit_cast(unsigned, v);
}
__device__ __forceinline__ float bflo(unsigned u) { return __uint_as_float(u << 16); }
__device__ __forceinline__ float bfhi(unsigned u) { return __uint_as_float(u & 0xffff0000u); }

template <int OFF> __device__ __forceinline__ void setm0(unsigned base) {
    asm volatile("s_add_u32 m0, %0, %1" :: "s"(base), "n"(OFF) : "scc");
}
__device__ __forceinline__ void dma_v(const void* g) { asm volatile("global_load_lds_dwordx4 %0, off" :: "v"(g) : "memory"); }
template <int IO> __device__ __forceinline__ void dma_s(const void* sb, unsigned vo) {
    asm volatile("global_load_lds_dwordx4 %0, %1 offset:%2" :: "v"(vo), "s"(sb), "n"(IO) : "memory");
}
template <int IO> __device__ __forceinline__ void dma_s_sc1(const void* sb, unsigned vo) {
    asm volatile("global_load_lds_dwordx4 %0, %1 offset:%2 sc1" :: "v"(vo), "s"(sb), "n"(IO) : "memory");
}
template <int IO> __device__ __forceinline__ void dma_s32_sc1(const void* sb, unsigned vo) {
    asm volatile("s_mov_b64 exec, %3\n\tglobal_load_lds_dwordx4 %0, %1 offset:%2 sc1\n\ts_mov_b64 exec, -1" :: "v"(vo), "s"(sb), "n"(IO), "s"(0xffffffffull) : "memory");
}
__device__ __forceinline__ void touch_s(const void* sb, unsigned vo) {
    asm volatile("global_load_lds_dword %0, %1" :: "v"(vo), "s"(sb) : "memory");
}
__device__ __forceinline__ unsigned ld_sc1(const unsigned* p) {
    unsigned a; asm volatile("global_load_dword %0, %1, off sc1\n\ts_waitcnt vmcnt(0)" : "=&v"(a) : "v"(p) : "memory"); return a;
}
__device__ __forceinline__ void nop1() { asm volatile("s_nop 0"); }
__device__ __forceinline__ void touch_v(const void* gaddr, unsigned ldsaddr) {
    asm volatile("s_mov_b32 m0, %1\n\ts_nop 0\n\tglobal_load_lds_dword %0, off" :: "v"(gaddr), "s"(ldsaddr) : "memory");
}
__device__ __forceinline__ void touch_v_sc1(const void* gaddr, unsigned ldsaddr) {
    asm volatile("s_mov_b32 m0, %1\n\ts_nop 0\n\tglobal_load_lds_dword %0, off sc1" :: "v"(gaddr), "s"(ldsaddr) : "memory");
}
template <int N> __device__ __forceinline__ void wait_vm() { asm volatile("s_waitcnt vmcnt(%0)" :: "n"(N) : "memory"); }
template <int N> __device__ __forceinline__ void wait_bar() {
    __builtin_amdgcn_s_waitcnt(0xC07F);
    asm volatile("s_waitcnt vmcnt(%0)\n\ts_barrier" :: "n"(N) : "memory");
}
__device__ __forceinline__ void bar_lgkm() { __builtin_amdgcn_s_waitcnt(0xC07F); asm volatile("s_barrier" ::: "memory"); }
__device__ __forceinline__ void bar() { asm volatile("s_barrier" ::: "memory"); }
__device__ __forceinline__ void ld2_sc1(unsigned& a, const unsigned* pa, unsigned& b, const unsigned* pb) {
    asm volatile("global_load_dword %0, %2, off sc1\n\tglobal_load_dword %1, %3, off sc1\n\ts_waitcnt vmcnt(0)"
                 : "=&v"(a), "=&v"(b) : "v"(pa), "v"(pb) : "memory");
}
__device__ __forceinline__ void st_sc1(unsigned* p, unsigned v, unsigned long long mask) {
    asm volatile("s_mov_b64 exec, %2\n\ts_cbranch_execz 1f\n\tglobal_store_dword %0, %1, off sc1\n1:\n\ts_mov_b64 exec, -1"
                 :: "v"(p), "v"(v), "s"(mask) : "memory");
}

__device__ __forceinline__ bf16x8 lds_a(unsigned addr) { return LDS(const bf16x8, addr); }
__device__ __forceinline__ bf16x8 lds_b(unsigned addr) {
    s16x4 lo = __builtin_amdgcn_ds_read_tr16_b64_v4i16((LAS s16x4*)(uintptr_t)addr);
    s16x4 hi = __builtin_amdgcn_ds_read_tr16_b64_v4i16((LAS s16x4*)(uintptr_t)(addr + 512));
    s16x8 r = __builtin_shufflevector(lo, hi, 0, 1, 2, 3, 4, 5, 6, 7);
    return __builtin_bit_cast(bf16x8, r);
}
__device__ __forceinline__ f32x4 mfma32(bf16x8 a, bf16x8 b, f32x4 c) { return __builtin_amdgcn_mfma_f32_16x16x32_bf16(a, b, c, 0, 0, 0); }
__device__ __forceinline__ void swap16(unsigned& a, unsigned& b) { auto r = __builtin_amdgcn_permlane16_swap(a, b, false, false); a = r[0]; b = r[1]; }
__device__ __forceinline__ void swap32(unsigned& a, unsigned& b) { auto r = __builtin_amdgcn_permlane32_swap(a, b, false, false); a = r[0]; b = r[1]; }
__device__ __forceinline__ void tr4(unsigned (&v)[4]) { swap16(v[0], v[1]); swap16(v[2], v[3]); swap32(v[0], v[2]); swap32(v[1], v[3]); }
__device__ __forceinline__ float xadd16(float v) { unsigned a = __float_as_uint(v), b = a; swap16(a, b); return __uint_as_float(a) + __uint_as_float(b); }
template <int CTRL> __device__ __forceinline__ float dadd(float v) {
    return v + __int_as_float(__builtin_amdgcn_update_dpp(0, __float_as_int(v), CTRL, 0xf, 0xf, true));
}
__device__ __forceinline__ float xadd32(float v) { unsigned a = __float_as_uint(v), b = a; swap32(a, b); return __uint_as_float(a) + __uint_as_float(b); }
__device__ __forceinline__ float red8(float v) { return dadd<0x141>(dadd<0x4E>(dadd<0xB1>(v))); }
__device__ __forceinline__ float red64(float v) { return xadd32(xadd16(dadd<0x140>(red8(v)))); }

constexpr int NB = 5, NA = 3;
constexpr int BBLK = 20480, ABLK = 18432;
constexpr int L_B = 0, L_A = NB * BBLK, L_ST = L_A + NA * ABLK;
constexpr int L_CNT = L_ST;
constexpr int L_DUMP = L_ST + 64;
constexpr int L_FE = L_DUMP + 256, L_FP = L_FE + 256;
constexpr int K_LDS = L_FP + 256;
constexpr unsigned SPIN_CAP = 1u << 16;
constexpr int W2_AT = 4;
#define CODE_LINES 246

__device__ __forceinline__ void ld_row(unsigned dst, const u16* x, int p, int ir) {
    int gr = p * 128 - 16 + ir; gr = gr < 0 ? 0 : gr;
    setm0<0>(dst); nop1(); dma_s<0>(x + (size_t)gr * 512, (threadIdx.x & 63) * 16);
}
__device__ __forceinline__ void ln_row(u32x4 v, u16* xn, int p, int ir, bool real) {
    float f[8], s = 0;
#pragma unroll
    for (int e = 0; e < 4; ++e) { const unsigned u = v[e]; f[2 * e] = bflo(u); f[2 * e + 1] = bfhi(u); s += f[2 * e] + f[2 * e + 1]; }
    const float sh = __int_as_float(__builtin_amdgcn_readfirstlane(__float_as_int(f[0])));
    float q = 0;
#pragma unroll
    for (int e = 0; e < 8; ++e) { const float d = f[e] - sh; q = fmaf(d, d, q); }
    const float mean = red64(s) * (1.f / 512.f);
    const float dm = mean - sh;
    const float var = fmaxf(red64(q) * (1.f / 512.f) - dm * dm, 0.f);
    const float sd = rbf(__builtin_amdgcn_sqrtf(rbf(rbf(var) + 1e-5f)));
    const float mb = rbf(mean), rc = __builtin_amdgcn_rcpf(sd);
    float o[8];
#pragma unroll
    for (int e = 0; e < 8; ++e) {
        const float a = rbf(f[e] - mb);
        const float q0 = a * rc;
        o[e] = fmaf(fmaf(-q0, sd, a), rc, q0);
    }
    const u32x4 ov = {pk2(o[0], o[1]), pk2(o[2], o[3]), pk2(o[4], o[5]), pk2(o[6], o[7])};
    const __amdgpu_buffer_rsrc_t xr = __builtin_amdgcn_make_buffer_rsrc(xn, 0, 16 * 148 * 1024, 0x00020000);
    if (real) __builtin_amdgcn_raw_buffer_store_b128(ov, xr, (unsigned)((p * 148 + ir) * 1024 + (threadIdx.x & 63) * 16), 0, 16);
    else asm volatile("" :: "v"(ov));
}
__device__ __forceinline__ void ln_done() {
    wait_vm<0>();
    if ((threadIdx.x & 63) == 0) __hip_atomic_fetch_add((LAS unsigned*)(uintptr_t)L_CNT, 1u, __ATOMIC_RELAXED, __HIP_MEMORY_SCOPE_WORKGROUP);
}

__device__ __forceinline__ void ln_exchange(u16* xn, unsigned* inbox, int w, int p, int j0) {
    const int l = threadIdx.x & 63;
    const unsigned* in = inbox + j0 * 32 + (l & 31);
    unsigned* outp = inbox + (l & 15) * 32 + ((l & 16) ? j0 : 16 + j0);
    unsigned want = 0;
    const int npass = (w == 0) ? 3 : 2;
#pragma unroll 1
    for (int pass = 1; pass < npass; ++pass) {
        if (pass == 1) {
            if (w < 4) wait_vm<0>(); else wait_vm<5>();
            if (w == 3) {
                want = LDS(const volatile unsigned, L_FP + l * 4) + 1u;
                st_sc1(outp, want, 0xffff0000ull);
            }
        }
        const u32x4 v = LDS(const volatile u32x4, L_A + (pass == 2 ? 8 : w) * 1024 + l * 16);
        ln_row(v, xn, p, pass == 2 ? j0 : 16 + 8 * j0 + w, pass != 0);
    }
    ln_done();
    if (w != 3) return;
    unsigned u = ld_sc1(in);
    unsigned spin = 0;
#pragma unroll 1
    while (__builtin_amdgcn_readfirstlane(LDS(const volatile unsigned, L_CNT)) != 8u && ++spin < (SPIN_CAP << 4)) {}
    st_sc1(outp, u, 0xffffull);
    bool go = false;
#pragma unroll 1
    for (spin = 0; spin < SPIN_CAP; ++spin) {
        const unsigned echoed = u;
        u = ld_sc1(in);
        st_sc1(outp, u, __ballot(u != echoed) & 0xffffull);
        if (go) break;
        if ((__ballot(u != want) & 0xffff0000ull) == 0ull) { go = true; bar(); }
    }
    if (!go) bar();
}
__device__ __forceinline__ void store_h(u16* a2, bool TA, int p, int j0) {
    int tid = threadIdx.x;
    asm volatile("" : "+v"(tid));
    const __amdgpu_buffer_rsrc_t a2r = __builtin_amdgcn_make_buffer_rsrc(a2, 0, S_ * A2LD * 2, 0x00020000);
    if (TA) {
        const int t4 = tid & 255;
        const unsigned rd = L_A + (t4 >> 3) * 144 + (t4 & 7) * 16;
        const unsigned go = ((p * 128 + (t4 >> 3)) * A2LD + 512 + 64 * j0 + (t4 & 7) * 8) * 2;
        bar_lgkm();
        u32x4 v[4];
#pragma unroll
        for (int it = 0; it < 4; ++it) v[it] = LDS(const u32x4, rd + it * 32 * 144);
#pragma unroll
        for (int it = 0; it < 4; ++it) __builtin_amdgcn_raw_buffer_store_b128(v[it], a2r, go, it * 32 * A2LD * 2, 16);
    } else {
        unsigned rd[5], go[5];
#pragma unroll
        for (int it = 0; it < 5; ++it) {
            const int id = it * 512 + tid, r_ = id / 20, c_ = id - r_ * 20;
            rd[it] = L_A + r_ * 336 + c_ * 16;
            go[it] = ((p * 128 + r_) * A2LD + 1024 + 160 * (j0 - 8) + c_ * 8) * 2;
        }
        bar_lgkm();
        u32x4 v[5];
#pragma unroll
        for (int it = 0; it < 5; ++it) v[it] = LDS(const u32x4, rd[it]);
#pragma unroll
        for (int it = 0; it < 5; ++it) __builtin_amdgcn_raw_buffer_store_b128(v[it], a2r, go[it], 0, 16);
    }
    if ((tid & 63) == 0) __hip_atomic_fetch_add((LAS unsigned*)(uintptr_t)(L_CNT + 12), 1u, __ATOMIC_RELAXED, __HIP_MEMORY_SCOPE_WORKGROUP);
    wait_vm<0>();
    if ((tid & 63) == 0) __hip_atomic_fetch_add((LAS unsigned*)(uintptr_t)(L_CNT + 4), 1u, __ATOMIC_RELAXED, __HIP_MEMORY_SCOPE_WORKGROUP);
}
__device__ __forceinline__ void lds_wait(int addr, unsigned val) {
    unsigned spin = 0;
#pragma unroll 1
    while (__builtin_amdgcn_readfirstlane(LDS(const volatile unsigned, addr)) != val && ++spin < (SPIN_CAP << 6)) {}
}

template <bool TA>
__device__ __forceinline__ void p1_mfma(u16* a2, int p, int j0) {
    int l = threadIdx.x & 63;
    asm volatile("" : "+v"(l));
    const int j = __builtin_amdgcn_readfirstlane(threadIdx.x >> 6);
    const int g = l >> 4, tok = l & 15;
    const unsigned adA0 = L_A + tok * 128 + ((g ^ (tok >> 1)) * 16);
    const int f2 = ((tok >> 3) & 1) | ((g & 1) << 1);
    const unsigned comB = L_B + (8 * g + (tok >> 2)) * 128;
    unsigned adB0, adB1 = 0;
    {
        const int pp = l & 3;
        if (TA) adB0 = comB + ((j ^ f2) * 32) + pp * 8;
        else {
            adB0 = comB + (j >> 1) * 4096 + (((2 * (j & 1)) ^ f2) * 32) + pp * 8;
            adB1 = comB + 2 * 4096 + ((((pp >> 1) * 2 + (j >> 1)) ^ f2) * 32) + (j & 1) * 16 + (pp & 1) * 8;
        }
    }
    f32x4 acc[8][5], acch[2];
#pragma unroll
    for (int i = 0; i < 8; ++i)
#pragma unroll
        for (int c = 0; c < 5; ++c) { acc[i][c] = f32x4{0, 0, 0, 0}; asm volatile("" : "+v"(acc[i][c])); }
    acch[0] = acch[1] = f32x4{0, 0, 0, 0};
    asm volatile("" : "+v"(acch[0]), "+v"(acch[1]));
    bf16x8 fa[6], fb[5];
    unsigned ra = adA0, rb0 = adB0, rb1 = adB1, rb2 = adB0 ^ 32;
    unsigned bo = 0, ao = 0;
    auto rdB = [&](auto R) {
        constexpr int r = R;
        if constexpr (TA) fb[r] = lds_b(rb0 + r * 4096);
        else fb[r] = (r == 0) ? lds_b(rb0) : (r == 1) ? lds_b(rb2) : (r == 2) ? lds_b(rb0 + 3 * 4096) : (r == 3) ? lds_b(rb2 + 3 * 4096) : lds_b(rb1);
    };
    auto mm = [&](auto I, auto C, auto SL) {
        constexpr int i = I, c = C, sl = SL;
        if constexpr (TA && c == 2) acc[i][c] = mfma32(fa[sl], fb[c], acc[i][c]);
        else acc[i][c] = mfma32(fb[c], fa[sl], acc[i][c]);
    };
    auto step = [&](auto PAR) {
        constexpr int par = PAR, X = par ? 3 : 0, Y = par ? 0 : 3;
        sfor<15>([&](auto I) {
            constexpr int idx = I, c = idx / 3, i = idx % 3;
            mm(IC(i), IC(c), IC(X + i));
            if constexpr (idx == 0) fa[Y] = lds_a(ra + 4 * 2048);
            if constexpr (idx == 1) fa[Y + 1] = lds_a(ra + 5 * 2048);
            if constexpr (idx == 2 && TA) fa[Y + 2] = lds_a(ra);
            if constexpr (idx == 4) { bo = (bo == (NB - 1) * BBLK) ? 0u : bo + BBLK; if constexpr (par == 1) ao = (ao == (NA - 1) * ABLK) ? 0u : ao + ABLK; asm volatile("" : "+s"(bo), "+s"(ao)); }
            if constexpr (idx == 6) { rb0 = adB0 + bo; asm volatile("" : "+v"(rb0)); }
            if constexpr (idx == 8 && !TA) { rb1 = adB1 + bo; asm volatile("" : "+v"(rb1)); }
            if constexpr (idx == 10 && !TA) { rb2 = rb0 ^ 32; asm volatile("" : "+v"(rb2)); }
            SB();
        });
        sfor<10>([&](auto I) {
            constexpr int idx = I, c = idx / 2, i = idx % 2;
            mm(IC(3 + i), IC(c), IC(Y + i));
            if constexpr (idx < 3) fa[X + idx] = lds_a(ra + (6 + idx) * 2048);
            if constexpr (idx == 4) { ra = (par == 0) ? (ra ^ 64) : adA0 + ao; asm volatile("" : "+v"(ra)); }
            SB();
        });
        if constexpr (TA) {
            acch[0] = mfma32(fb[1], fa[Y + 2], acch[0]);
            acch[1] = mfma32(fa[Y + 2], fb[2], acch[1]);
        }
        SB();
        bar_lgkm();
        SB();
        sfor<5>([&](auto C) {
            constexpr int c = C;
            mm(IC(5), C, IC(X));
            if constexpr (c < 3) fa[Y + c] = lds_a(ra + (1 + c) * 2048);
            SB();
            mm(IC(6), C, IC(X + 1));
            mm(IC(7), C, IC(X + 2));
            SB();
            rdB(C);
            SB();
        });
    };
    __builtin_amdgcn_s_setprio(1);
    SB();
    bar_lgkm();
    SB();
    sfor<5>([&](auto C) {
        constexpr int c = C;
        if constexpr (c < 3) fa[c] = lds_a(ra + (1 + c) * 2048);
        SB();
        rdB(C);
        SB();
    });
#pragma unroll 1
    for (int s = 0; s < 16; s += 2) {
        step(IC(0));
        step(IC(1));
    }
    __builtin_amdgcn_s_setprio(0);
    constexpr int OST = TA ? 144 : 336;
    const unsigned ow = L_A + tok * OST + g * 8;
    auto cvt4 = [&](f32x4 a) { return u32x2{pk2(a[0], a[1]), pk2(a[2], a[3])}; };
    auto hmul = [&](f32x4 ga, f32x4 ua) {
        f32x4 t;
#pragma unroll
        for (int e = 0; e < 4; ++e) t[e] = fmaxf(ga[e], 0.f);
        return cvt4(t * ua);
    };
    if (TA) {
#pragma unroll
        for (int t = 0; t < 8; ++t) LDS(u32x2, ow + 16 * t * OST + j * 32) = hmul(acc[t][3], acc[t][4]);
        bar_lgkm();
        const unsigned oo = L_B + 16384 + tok * 128 + (((2 * j + (g >> 1)) ^ (tok & 7)) * 16) + (g & 1) * 8;
        const bool par = tok & 1;
        const int da = tok - (4 * g + (tok & 1));
        const bool ca = da >= 0, cb = da >= 2;
        const f32x4 z = {0, 0, 0, 0};
        s16x4 kprev = __builtin_bit_cast(s16x4, cvt4(acch[0]));
        u32x2 vprev = cvt4(acch[1]);
#pragma unroll
        for (int hb = 0; hb < 2; ++hb) {
            s16x4 kf[5], qf[4]; u32x2 vf[5];
            kf[0] = kprev; vf[0] = vprev;
#pragma unroll
            for (int i = 0; i < 4; ++i) {
                kf[i + 1] = __builtin_bit_cast(s16x4, cvt4(acc[4 * hb + i][1]));
                qf[i] = __builtin_bit_cast(s16x4, cvt4(acc[4 * hb + i][0]));
            }
            f32x4 sp[4], sc[4];
#pragma unroll
            for (int i = 0; i < 4; ++i) {
                sp[i] = __builtin_amdgcn_mfma_f32_16x16x16bf16_1k(kf[i], qf[i], z, 0, 0, 0);
                sc[i] = __builtin_amdgcn_mfma_f32_16x16x16bf16_1k(kf[i + 1], qf[i], z, 0, 0, 0);
            }
#pragma unroll
            for (int i = 0; i < 4; ++i) vf[i + 1] = cvt4(acc[4 * hb + i][2]);
            kprev = kf[4]; vprev = vf[4];
            unsigned lw[4];
#pragma unroll
            for (int i = 0; i < 4; ++i) {
                asm volatile("" : "+v"(sp[i]), "+v"(sc[i]));
                const float a0 = par ? sc[i][1] : sc[i][0], a1 = par ? sp[i][1] : sp[i][0];
                const float b0 = par ? sc[i][3] : sc[i][2], b1 = par ? sp[i][3] : sp[i][2];
                float la = ca ? a0 : a1, lb = cb ? b0 : b1;
                if (i == 0 && hb == 0) { if (p == 0) { la = ca ? la : -3.0e38f; lb = cb ? lb : -3.0e38f; } }
                lw[i] = pk2(la, lb);
            }
            tr4(lw);
            float e8[8];
#pragma unroll
            for (int i = 0; i < 4; ++i) { e8[2 * i] = bflo(lw[i]); e8[2 * i + 1] = bfhi(lw[i]); }
            const float mx = fmaxf(fmaxf(fmaxf(e8[0], e8[1]), fmaxf(e8[2], e8[3])), fmaxf(fmaxf(e8[4], e8[5]), fmaxf(e8[6], e8[7])));
            float sum = 0;
#pragma unroll
            for (int i = 0; i < 8; ++i) { e8[i] = __builtin_amdgcn_exp2f((e8[i] - mx) * (0.25f * LOG2E)); sum += e8[i]; }
            const float inv = __builtin_amdgcn_rcpf(sum);
#pragma unroll
            for (int i = 0; i < 4; ++i) lw[i] = pk2(e8[2 * i] * inv, e8[2 * i + 1] * inv);
            tr4(lw);
#pragma unroll
            for (int i = 0; i < 4; ++i) {
                const unsigned pw = lw[i];
                const unsigned wa = par ? (pw << 16) : (pw & 0xffffu), wb = par ? (pw & 0xffff0000u) : (pw >> 16);
                const u32x4 pp = {ca ? 0u : wa, cb ? 0u : wb, ca ? wa : 0u, cb ? wb : 0u};
                const u32x4 vv = {vf[i][0], vf[i][1], vf[i + 1][0], vf[i + 1][1]};
                sp[i] = mfma32(__builtin_bit_cast(bf16x8, vv), __builtin_bit_cast(bf16x8, pp), z);
            }
#pragma unroll
            for (int i = 0; i < 4; ++i) LDS(u32x2, oo + ((4 * hb + i) >> 1) * BBLK + (i & 1) * 2048) = cvt4(sp[i]);
        }
    } else {
        const int jj = j0 - 8, odd = jj & 1;
        const int lo_off = (odd ? 64 : 0) + j * 64;
        const int mx_off = (odd ? 0 : 256) + j * 16;
#pragma unroll
        for (int i = 0; i < 8; ++i)
#pragma unroll
            for (int c = 0; c < 2; ++c) LDS(u32x2, ow + 16 * i * OST + lo_off + c * 32) = hmul(acc[i][c], acc[i][c + 2]);
#pragma unroll
        for (int i = 0; i < 8; i += 2) {
            f32x4 ga, ua;
#pragma unroll
            for (int e = 0; e < 4; ++e) {
                unsigned a = __float_as_uint(acc[i][4][e]), b = __float_as_uint(acc[i + 1][4][e]);
                swap32(a, b);
                ga[e] = __uint_as_float(a); ua[e] = __uint_as_float(b);
            }
            LDS(u32x2, L_A + (16 * (i + (g >> 1)) + tok) * OST + mx_off + (g & 1) * 8) = hmul(ga, ua);
        }
        store_h(a2, false, p, j0);
    }
}

constexpr int a2slot(int st) { return (st & 3) == 3 ? L_B : L_A + (st & 3) * ABLK; }
constexpr int cnt_iv(int t) { return t < 12 ? ((t & 1) ? 7 : 8) : (t == 12 ? 9 : 4); }
constexpr int wait_iv(int t) {
    return t < 2 ? 15 : (t == 14 ? 4 : 0) + cnt_iv(t - 1) + cnt_iv(t);
}

__global__ __launch_bounds__(512, 2) void kfused(const u16* x, const u16* wqkv, const u16* wo, const u16* wg, const u16* wu, const u16* wd,
                                                 u16* a2, u16* out, unsigned* flags) {
    const int w = __builtin_amdgcn_readfirstlane(threadIdx.x >> 6);
    const int b = blockIdx.x, xcd = b & 7, loc = b >> 3;
    if (w == 7) {
        unsigned long long pc;
        asm volatile("s_getpc_b64 %0" : "=s"(pc));
        const int l = threadIdx.x & 63;
        int ln = (l < 32) ? l : 32 + loc * 7 + ((l - 32) % 7);
        ln = ln < CODE_LINES ? ln : 0;
        asm volatile("s_mov_b32 m0, %0\n\ts_nop 0" :: "s"(L_DUMP));
        touch_s((const char*)(pc & ~127ull), (unsigned)(ln * 128));
    }
    const int ci = loc >> 3;
    const int p = (xcd & 1) * 8 + (loc & 7), qd = xcd >> 1;
    const int j0 = (ci < 2) ? 2 * qd + ci : 6 + 2 * qd + ci;
    const int rt = (xcd & 1) * 16 + (xcd >> 1) * 4 + (loc & 3), ct = loc >> 2;
    const int R0 = rt * 64, C0 = ct * 64;
    const bool TA = ci < 2;
    u16* const xn = a2 + (5 << 20);
    unsigned* const inbox = (unsigned*)(xn + (size_t)(p * 148 + 144) * 512);
    ld_row(L_A + w * 1024, x, p, 16 + 8 * j0 + w);
    if (w == 0) ld_row(L_A + 8192, x, p, j0);
    if (w == 3) touch_v_sc1(inbox + j0 * 32 + (threadIdx.x & 31), L_FP);
    if (threadIdx.x < 2) LDS(volatile u32x4, L_CNT + threadIdx.x * 16) = u32x4{0u, 0u, 0u, 0u};
    bar_lgkm();

    const int h = w & 3;
    const int l = threadIdx.x & 63, kq = l >> 3;
    const int lchB = (l & 7) ^ ((((kq >> 1) & 1) | ((h & 1) << 1)) << 1);
    const char *sB0, *sB1, *sB3, *sB4, *gM; unsigned vB0, vB1, st0;
    {
        const int k = 8 * h + kq;
        const int jj = j0 - 8, f0 = 512 + 160 * jj, odd = jj & 1;
        st0 = TA ? 3072u : (unsigned)(F_ * 2);
        vB1 = (unsigned)(k * (F_ * 2) + lchB * 16);
        vB0 = (unsigned)(k * st0 + lchB * 16);
        sB0 = TA ? (const char*)(wqkv + 64 * j0) : (const char*)(wg + f0 + (odd ? 32 : 0));
        sB1 = sB0 + (TA ? 1024 : 128);
        sB3 = TA ? (const char*)(wg + 64 * j0) : (const char*)(wu + f0 + (odd ? 32 : 0));
        sB4 = TA ? (const char*)(wu + 64 * j0) : sB3 + 128;
        const char* m0_ = (const char*)(wg + f0 + (odd ? 0 : 128)), *m1_ = (const char*)(wu + f0 + (odd ? 0 : 128)) - 64;
        gM = (TA ? sB0 + 2048 : (lchB < 4 ? m0_ : m1_)) + vB0;
    }
    const unsigned dB = L_B + h * 1024;
    auto issB = [&](auto SLOT) {
        constexpr int so = SLOT * BBLK;
        setm0<so>(dB); nop1(); dma_s<0>(sB0, vB0);
        setm0<so + 4096>(dB); nop1(); dma_s<0>(sB1, vB0);
        setm0<so + 8192>(dB); nop1(); dma_v(gM);
        setm0<so + 12288>(dB); nop1(); dma_s<0>(sB3, vB1);
        setm0<so + 16384>(dB); nop1(); dma_s<0>(sB4, vB1);
        vB1 += 32 * F_ * 2; vB0 += 32 * st0; gM += 32 * st0;
    };
    if (w >= 4) issB(IC(0));
    ln_exchange(xn, inbox, w, p, j0);

    if (w >= 4) {
        issB(IC(1));
        wait_vm<2>();
        issB(IC(2));
        unsigned* const myP = flags + 32768 + (p * 16 + (l & 15)) * 32 + j0;
        const unsigned* const myE = flags + 512 + (p * 16 + j0) * 32 + (l & 15);
        const unsigned* const pollP = flags + 32768 + ((rt >> 1) * 16 + (rt & 1) * 8 + ct) * 32 + (l & 15);
        unsigned* const echoE = flags + 512 + ((rt >> 1) * 16 + (l & 15)) * 32 + (rt & 1) * 8 + ct;
        unsigned myv = 0, last = 2;
        const char* xs = (const char*)(xn + (size_t)p * 148 * 512);
        unsigned vA[4], vH;
        {
            const int rr = 8 * h + kq;
            vA[0] = (unsigned)((16 + rr) * 1024 + (((l & 7) ^ ((rr >> 1) & 7)) * 16));
            vA[1] = vA[0] + 32768; vA[2] = vA[0] + 65536; vA[3] = vA[0] + 98304;
            const int hr = 4 * h + (kq & 3);
            vH = (unsigned)(hr * 1024 + (((l & 7) ^ ((hr >> 1) & 7)) * 16));
        }
        const unsigned dA = L_A + 2048 + h * 1024, dH = L_A + h * 512;
        auto issA = [&](auto BLK, auto HALF) {
            constexpr int S = BLK, hf = HALF, so = (S % NA) * ABLK - S * 128;
            setm0<so + hf * 8192>(dA); nop1(); dma_s_sc1<S * 128>(xs, vA[2 * hf]);
            setm0<so + hf * 8192 + 4096>(dA); nop1(); dma_s_sc1<S * 128>(xs, vA[2 * hf + 1]);
            if constexpr (hf == 0) { setm0<so>(dH); dma_s32_sc1<S * 128>(xs, vH); }
        };
        const unsigned vW = (unsigned)((8 * h + kq) * 1024 + lchB * 16);
        const char* const wdc = (const char*)(wd + C0);
        const char* const woc = (const char*)(wo + C0);
        auto issW = [&](auto ST) {
            constexpr int st = ST, so = (1 + (st & 3)) * BBLK;
            const char* base = (st < 14) ? wdc + st * 131072 : woc + (st - 14) * 131072;
            sfor<4>([&](auto C) { constexpr int c = C; setm0<so + c * 4096>(dB); nop1(); dma_s<0>(base + c * 32768, vW); });
        };
        auto warm2 = [&](auto R) {
            constexpr int r = R;
            if (h == ((r + 1) & 3)) {
                const int vr = (r * 32 + loc) * 8;
                const char* a = (r < 7) ? (const char*)wd + vr * 1024 : (const char*)wo + (vr - 1792) * 1024;
                touch_v(a + l * 128, L_DUMP);
            }
        };
        bar();
        issA(IC(0), IC(0)); issA(IC(0), IC(1));
        issA(IC(1), IC(0)); issA(IC(1), IC(1));
        wait_bar<5>();
        if (h == 0) touch_v_sc1(myE, L_FE);
        sfor<16>([&](auto T) {
            constexpr int t = T;
            if constexpr (t < 12) issA(IC(t / 2 + 2), IC(t & 1));
            if constexpr (t < 13) issB(IC((t + 3) % NB));
            if constexpr (t >= 12) issW(IC(t - 12));
            if constexpr (t >= W2_AT && t < W2_AT + 9) warm2(IC(t - W2_AT));
            if constexpr (t == 2) {
                if (h == 0) {
                    const unsigned ev = LDS(const volatile unsigned, L_FE + l * 4);
                    unsigned v = 0;
                    while (__ballot(ev == v) != 0ull) v += 4;
                    myv = v;
                    st_sc1(myP, v, 0xffffull);
                }
            }
            if constexpr (t == 6) { if (h == 0) touch_v_sc1(pollP, L_FP); }
            if constexpr (t == 10) {
                if (h == 0) {
                    const unsigned pv = LDS(const volatile unsigned, L_FP + l * 4);
                    st_sc1(echoE, pv, __ballot(!(pv & 3)) & 0xffffull);
                    last = (pv & 3) ? last : pv;
                }
            }
            if constexpr (t == 14) { if (h == 0) touch_v_sc1(myE, L_FE); }
            if constexpr (t < 15) wait_bar<wait_iv(t)>(); else bar();
        });
        unsigned vA2[2];
        { const int row = 8 * h + kq; vA2[0] = (unsigned)(row * (A2LD * 2) + (((l & 7) ^ ((row >> 1) & 7)) * 16)); vA2[1] = vA2[0] + 32 * A2LD * 2; }
        const char* const sa = (const char*)(a2 + (size_t)R0 * A2LD);
        const unsigned dA2 = h * 1024;
        store_h(a2, TA, p, j0);
        unsigned ev = 0, state = 0, shown = 0;
        auto serve = [&](unsigned pv, unsigned long long mask, unsigned ign, bool flush) {
#pragma unroll 1
            for (unsigned spin = 0; spin < SPIN_CAP; ++spin) {
                if (state == 0 && __builtin_amdgcn_readfirstlane(LDS(const volatile unsigned, L_CNT + 4)) == (TA ? 4u : 8u)) state = TA ? 1u : 3u;
                if (state != shown && __ballot(ev != myv) == 0ull) { st_sc1(myP, myv + state, 0xffffull); shown = state; }
                st_sc1(echoE, pv, __ballot(!(pv & 3) && pv != last) & 0xffffull);
                last = (pv & 3) ? last : pv;
                if ((__ballot(((pv - last) | ign) != 3u || (last & 3)) & mask) == 0ull && !(flush && shown != 3u)) break;
                ld2_sc1(pv, pollP, ev, myE);
            }
        };
        if (h == 0) {
            ev = LDS(const volatile unsigned, L_FE + l * 4);
            serve(last, 0xffull, 2u, false);
            if (l == 0) LDS(volatile unsigned, L_CNT + 8) = 1u;
        } else {
            lds_wait(L_CNT + 16, 4u);
            const unsigned vW0 = (unsigned)(kq * 1024 + (((l & 7) ^ (((kq >> 1) & 1) << 1)) * 16));
            sfor<4>([&](auto S4) {
                constexpr int st = 4 + S4;
                issW(IC(st));
                if (h == 1 + (S4 % 3)) sfor<4>([&](auto C) { constexpr int c = C; setm0<(1 + (st & 3)) * BBLK + c * 4096>(0u); nop1(); dma_s<0>(wdc + st * 131072 + c * 32768, vW0); });
            });
        }
        lds_wait(L_CNT + 12, TA ? 4u : 8u);
        lds_wait(L_CNT + 8, 1u);
        __builtin_amdgcn_s_setprio(2);
        auto issA2 = [&](auto ST) {
            constexpr int st = ST, ko = (st < 14) ? 1024 + st * 256 : (st - 14) * 256, so = a2slot(st);
            const char* sk = sa + ko;
            setm0<so>(dA2); nop1(); dma_s_sc1<0>(sk, vA2[0]);
            setm0<so + 4096>(dA2); nop1(); dma_s_sc1<0>(sk, vA2[1]);
            setm0<so + 8192 - 128>(dA2); nop1(); dma_s_sc1<128>(sk, vA2[0]);
            setm0<so + 12288 - 128>(dA2); nop1(); dma_s_sc1<128>(sk, vA2[1]);
        };
        issA2(IC(0)); issA2(IC(1));
        if (h == 0) serve(last, 0ull, 0u, false);
        wait_vm<4>();
        bar();
        if (h == 0) { serve(last, 0ull, 0u, false); touch_v_sc1(pollP, L_FP); }
        if (TA) {
            const __amdgpu_buffer_rsrc_t a2r = __builtin_amdgcn_make_buffer_rsrc(a2, 0, S_ * A2LD * 2, 0x00020000);
            const int t4 = threadIdx.x & 255, r_ = t4 >> 3;
            const unsigned rd = L_B + 16384 + r_ * 128 + (((t4 & 7) ^ (r_ & 7)) * 16);
            const unsigned go = ((p * 128 + r_) * A2LD + 64 * j0 + (t4 & 7) * 8) * 2;
            u32x4 v[4];
#pragma unroll
            for (int it = 0; it < 4; ++it) v[it] = LDS(const u32x4, rd + it * BBLK);
#pragma unroll
            for (int it = 0; it < 4; ++it) __builtin_amdgcn_raw_buffer_store_b128(v[it], a2r, go, it * 32 * A2LD * 2, 16);
        }
        issA2(IC(2)); issA2(IC(3));
        if (TA) wait_vm<12>(); else wait_vm<8>();
        bar();
        sfor<16>([&](auto ST) {
            constexpr int st = ST;
            if constexpr (st == 0) {
                if (h == 0) {
                    serve(LDS(const volatile unsigned, L_FP + l * 4), 0xff00ull, 2u, false);
                    if (l == 0) LDS(volatile unsigned, L_CNT + 8) = 2u;
                }
                lds_wait(L_CNT + 8, 2u);
                issA2(IC(4));
            } else if constexpr (st < 14) { issA2(IC(st + 4)); if constexpr (st >= 4) issW(IC(st + 4)); }
            if constexpr (st == 1) { if (h == 0) { state = 3u; serve(last, 0ull, 0u, false); } }
            if constexpr (st == 6) { if (h == 0) touch_v_sc1(pollP, L_FP); }
            if constexpr (st == 9) { if (h == 0) serve(LDS(const volatile unsigned, L_FP + l * 4), 0xffull, 0u, true); }
            wait_bar<(st < 4) ? 8 : (st == 4) ? 12 : (st < 14) ? 16 : (st == 14) ? 8 : 0>();
        });
        return;
    }
    if (w < 3) {
        const int row = 96 + 52 * (loc & 7) + (l < 52 ? l : 51);
#pragma unroll
        for (int i = 0; i < 2; ++i) {
            const int t = 2 * w + i;
            const char* a;
            if (TA) a = (t < 3) ? (const char*)(wqkv + 512 * t + 64 * j0) + row * 3072 : (const char*)((t & 1 ? wg : wu) + 64 * j0) + row * (F_ * 2);
            else a = (const char*)((t & 1 ? wg : wu) + ((512 + 160 * (j0 - 8)) & ~63) + 64 * (t >> 1)) + row * (F_ * 2);
            touch_v(a, L_DUMP);
            if (i == 0) wait_vm<0>();
        }
        bar();
    }
    if (TA) p1_mfma<true>(a2, p, j0); else p1_mfma<false>(a2, p, j0);
    {
        int l2 = threadIdx.x & 63;
        asm volatile("" : "+v"(l2));
        const int g = l2 >> 4, tok = l2 & 15;
        const int wm = w >> 1, wn = w & 1;
        const unsigned a2A = L_A + (32 * wm + tok) * 128 + ((g ^ (tok >> 1)) * 16);
        const unsigned a2B = L_B + (8 * g + (tok >> 2)) * 128 + (((2 * wn) ^ (((tok >> 3) & 1) | ((g & 1) << 1))) * 32) + (l2 & 3) * 8;
        const size_t orow = (size_t)(R0 + 32 * wm + tok) * D_ + C0 + 32 * wn + 4 * g;
        u32x2 xv[2][2];
#pragma unroll
        for (int i = 0; i < 2; ++i)
#pragma unroll
            for (int c = 0; c < 2; ++c) xv[i][c] = *(const u32x2*)(x + orow + i * 16 * D_ + 16 * c);
        f32x4 c2[2][2][2];
#pragma unroll
        for (int a = 0; a < 2; ++a)
#pragma unroll
            for (int i = 0; i < 2; ++i)
#pragma unroll
                for (int c = 0; c < 2; ++c) c2[a][i][c] = f32x4{0, 0, 0, 0};
        bf16x8 ga[2][4][2], gb[2][4][2];
        const unsigned pa0 = a2A, pa1 = a2A ^ 64, pa2 = a2A - L_A, pa3 = pa2 ^ 64;
        const unsigned pb0 = a2B + BBLK, pb1 = pb0 ^ 32, pb2 = pb0 + 2 * BBLK, pb3 = pb2 ^ 32;
        auto rdA = [&](auto SET, auto I, auto Q) {
            constexpr int set = SET, i = I, ks = i >> 1, q = Q;
            const unsigned ab = (q == 3) ? ((ks & 1) ? pa3 : pa2) : ((ks & 1) ? pa1 : pa0);
            ga[set][ks][i & 1] = lds_a(ab + (q == 3 ? 0 : q * ABLK) + (ks >> 1) * 8192 + (i & 1) * 2048);
        };
        auto rdB2 = [&](auto SET, auto I, auto Q) {
            constexpr int set = SET, i = I, ks = i >> 1, q = Q;
            const unsigned bb = (q >= 2) ? ((i & 1) ? pb3 : pb2) : ((i & 1) ? pb1 : pb0);
            gb[set][ks][i & 1] = lds_b(bb + (q & 1) * BBLK + ks * 4096);
        };
        auto stage = [&](auto Q, auto ACC, auto LAST) {
            constexpr int q = Q, set = q & 1, ac = ACC; constexpr bool lastst = LAST;
            if constexpr (!lastst) bar_lgkm();
            SB();
            sfor<16>([&](auto I) {
                constexpr int idx = I, ks = idx >> 2, i = (idx >> 1) & 1, c = idx & 1;
                c2[ac][i][c] = mfma32(gb[set][ks][c], ga[set][ks][i], c2[ac][i][c]);
                if constexpr (!lastst && idx < 12) {
                    constexpr int kk = idx / 3, r3 = idx % 3;
                    if constexpr (r3 == 0) { rdA(IC(set ^ 1), IC(2 * kk), IC((q + 1) & 3)); rdA(IC(set ^ 1), IC(2 * kk + 1), IC((q + 1) & 3)); }
                    else rdB2(IC(set ^ 1), IC(2 * kk + r3 - 1), IC((q + 1) & 3));
                }
                SB();
            });
        };
        typedef std::integral_constant<bool, true> T_; typedef std::integral_constant<bool, false> F_t;
        bf16x8 gp[4][4][2];
        lds_wait(L_CNT + 4, TA ? 4u : 8u);
        sfor<32>([&](auto I) {
            constexpr int q = I >> 3, ks = (I >> 1) & 3, c = I & 1;
            gp[q][ks][c] = lds_b(((q >= 2) ? (c ? pb3 : pb2) : (c ? pb1 : pb0)) + (q & 1) * BBLK + ks * 4096);
        });
        __builtin_amdgcn_s_waitcnt(0xC07F);
        asm volatile("" ::: "memory");
        if (l2 == 0) __hip_atomic_fetch_add((LAS unsigned*)(uintptr_t)(L_CNT + 16), 1u, __ATOMIC_RELAXED, __HIP_MEMORY_SCOPE_WORKGROUP);
        asm volatile("s_waitcnt vmcnt(0)" : "+v"(xv[0][0]), "+v"(xv[0][1]), "+v"(xv[1][0]), "+v"(xv[1][1]) :: "memory");
        bar_lgkm();
        sfor<8>([&](auto I) { rdA(IC(0), I, IC(0)); });
        sfor<4>([&](auto Q) {
            constexpr int q = Q, set = q & 1;
            bar_lgkm();
            SB();
            sfor<16>([&](auto I) {
                constexpr int idx = I, ks = idx >> 2, i = (idx >> 1) & 1, c = idx & 1;
                c2[1][i][c] = mfma32(gp[q][ks][c], ga[set][ks][i], c2[1][i][c]);
                if constexpr (idx < 8) { rdA(IC(set ^ 1), I, IC((q + 1) & 3)); if constexpr (q == 3) rdB2(IC(0), I, IC(0)); }
                SB();
            });
        });
#pragma unroll 1
        for (int it = 1; it < 3; ++it) {
            stage(IC(0), IC(1), F_t{}); stage(IC(1), IC(1), F_t{}); stage(IC(2), IC(1), F_t{}); stage(IC(3), IC(1), F_t{});
        }
        stage(IC(0), IC(1), F_t{}); stage(IC(1), IC(1), F_t{});
        stage(IC(2), IC(0), F_t{}); stage(IC(3), IC(0), F_t{});
        stage(IC(0), IC(0), F_t{}); stage(IC(1), IC(0), T_{});
#pragma unroll
        for (int i = 0; i < 2; ++i)
#pragma unroll
            for (int c = 0; c < 2; ++c) {
                float r[4];
#pragma unroll
                for (int e = 0; e < 4; ++e) {
                    const float d = rbf(c2[1][i][c][e]);
                    const float sg = rbf(d * __builtin_amdgcn_rcpf(1.f + __builtin_amdgcn_exp2f(-d * LOG2E)));
                    const float t = rbf(rbf(c2[0][i][c][e]) * sg);
                    const float xx = (e & 1) ? bfhi(xv[i][c][e >> 1]) : bflo(xv[i][c][e >> 1]);
                    r[e] = xx + t;
                }
                *(u32x2*)(out + orow + i * 16 * D_ + 16 * c) = u32x2{pk2(r[0], r[1]), pk2(r[2], r[3])};
            }
    }
}

extern "C" void kernel_launch(void* const* d_in, const int* in_sizes, int n_in,
                              void* d_out, int out_size, void* d_ws, size_t ws_size,
                              hipStream_t stream) {
    (void)hipFuncSetAttribute((const void*)kfused, hipFuncAttributeMaxDynamicSharedMemorySize, K_LDS);
    kfused<<<256, 512, K_LDS, stream>>>((const u16*)d_in[0], (const u16*)d_in[1], (const u16*)d_in[2], (const u16*)d_in[4], (const u16*)d_in[5],
                                        (const u16*)d_in[6], (u16*)d_ws, (u16*)d_out, (unsigned*)((char*)d_ws + (16 << 20)));
}
```
